```python
import torch

def _softmax(x, axis=-1):
    return torch.softmax(x, dim=axis)
def _gelu(x):
    return torch.nn.functional.gelu(x, approximate="tanh")
def _silu(x):
    return torch.nn.functional.silu(x)
def _sigmoid(x):
    return torch.sigmoid(x)
def _layernorm(x, axis=-1, eps=1e-5):
    m = x.mean(dim=axis, keepdim=True)
    v = x.var(dim=axis, keepdim=True, unbiased=False)
    return (x - m) / torch.sqrt(v + eps)
def _scatter_add(tgt, idx, src, axis=0):
    return tgt.clone().index_add_(axis, idx, src)
def _segment_sum(src, seg, n):
    out = torch.zeros((n,) + src.shape[1:], dtype=src.dtype)
    return out.index_add_(0, seg, src)
def _pad(x, pw):
    flat = tuple(v for pair in reversed(pw) for v in pair)
    return torch.nn.functional.pad(x, flat)

kernel_name = 'sequential_preLN__linear_kernel_hedgehog__dense_activation_gelu__prompt'

def setup_inputs():

    _w = (lambda a: (lambda t: t.to(torch.bfloat16) if t.is_floating_point() else t)(torch.as_tensor(a)))
    def _A_setup():
        B, S, D, H = 1, 2048, 512, 32
        Dh = D // H
        return {
            "x": _w((lambda *s: torch.randn(*s, dtype=torch.bfloat16) * 0.05)(B, S, D)),
            "wqkv": _w((lambda *s: torch.randn(*s, dtype=torch.bfloat16) * 0.2)(D, 3 * D)),
            "w_phi": _w((lambda *s: torch.randn(*s, dtype=torch.bfloat16) * 0.2)(H, Dh, Dh) * (Dh ** -0.5)),
            "wo": _w((lambda *s: torch.randn(*s, dtype=torch.bfloat16) * 0.05)(D, D)),
        }
    _A_in = _A_setup()

    def _B_setup():
        B, S, D, F = 1, 2048, 512, 1376
        return {
            "x": _w((lambda *s: torch.randn(*s, dtype=torch.bfloat16) * 0.05)(B, S, D)),
            "w1": _w((lambda *s: torch.randn(*s, dtype=torch.bfloat16) * 1.6)(D, F) * (D ** -0.5)),
            "b1": _w((lambda *s: torch.randn(*s, dtype=torch.bfloat16) * 0.1)(F) * 0.01),
            "w2": _w((lambda *s: torch.randn(*s, dtype=torch.bfloat16) * 0.1)(F, D) * (F ** -0.5)),
            "b2": _w((lambda *s: torch.randn(*s, dtype=torch.bfloat16) * 0.1)(D) * 0.01),
        }
    _B_in = _B_setup()

    return {
        "A_x": _A_in["x"],
        "A_wqkv": _A_in["wqkv"],
        "A_w_phi": _A_in["w_phi"],
        "A_wo": _A_in["wo"],
        "B_x": _B_in["x"],
        "B_w1": _B_in["w1"],
        "B_b1": _B_in["b1"],
        "B_w2": _B_in["w2"],
        "B_b2": _B_in["b2"],
    }

def reference(A_x, A_wqkv, A_w_phi, A_wo, B_x, B_w1, B_b1, B_w2, B_b2):
    def _A_ref(x, wqkv, w_phi, wo):
        B, S, D = x.shape
        H = 32; Dh = D // H
        qkv = (lambda x, *d: x.permute(*d))((x @ wqkv).reshape(B, S, 3, H, Dh), 2, 0, 3, 1, 4)
        q, k, v = qkv[0], qkv[1], qkv[2]
        pq = torch.einsum("bhsd,hdf->bhsf", q, w_phi)
        pk = torch.einsum("bhsd,hdf->bhsf", k, w_phi)
        phi_q = torch.concatenate([_softmax(pq, axis=-1), _softmax(-pq, axis=-1)], axis=-1)
        phi_k = torch.concatenate([_softmax(pk, axis=-1), _softmax(-pk, axis=-1)], axis=-1)
        kv = torch.einsum("bhsf,bhsd->bhfd", phi_k, v)
        z  = torch.einsum("bhsf->bhf", phi_k)
        num = torch.einsum("bhqf,bhfd->bhqd", phi_q, kv)
        den = torch.einsum("bhqf,bhf->bhq", phi_q, z)[..., None] + 1e-6
        o = num / den
        return (lambda x, *d: x.permute(*d))(o, 0, 2, 1, 3).reshape(B, S, D) @ wo

    def _B_ref(x, w1, b1, w2, b2):
        h = torch.einsum("bsd,df->bsf", x, w1) + b1
        h = _gelu(h)
        return torch.einsum("bsf,fd->bsd", h, w2) + b2

    x = A_x
    h = x + _A_ref(_layernorm(x, axis=-1), A_wqkv, A_w_phi, A_wo)
    h2 = h + _B_ref(_layernorm(h, axis=-1), B_w1, B_b1, B_w2, B_b2)
    return h2
```

```cpp
#include <hip/hip_runtime.h>
#include <hip/hip_bf16.h>
#include <stdint.h>
typedef unsigned short u16;
__device__ __forceinline__ float bf2f(u16 v){ return __uint_as_float(((unsigned)v)<<16); }
__device__ __forceinline__ u16 f2bf(float f){ unsigned u=__float_as_uint(f); u += 0x7fffu + ((u>>16)&1u); return (u16)(u>>16); }
constexpr int S=2048,D=512,H=32,DH=16,F=1376;

__global__ void k_ln(const u16* x, float* y){
  int r=blockIdx.x, t=threadIdx.x; float v[8]; float s=0;
  for(int i=0;i<8;i++){ v[i]=bf2f(x[r*D+t*8+i]); s+=v[i]; }
  for(int o=32;o;o>>=1) s+=__shfl_xor(s,o);
  float m=s/D; float q=0; for(int i=0;i<8;i++){ float d=v[i]-m; q+=d*d; }
  for(int o=32;o;o>>=1) q+=__shfl_xor(q,o);
  float rs=rsqrtf(q/D+1e-5f);
  for(int i=0;i<8;i++) y[r*D+t*8+i]=(v[i]-m)*rs;
}
__global__ void k_gemm(const float* A, const u16* B, float* C, int M,int N,int K){
  int n=blockIdx.x*blockDim.x+threadIdx.x; int m=blockIdx.y; if(n>=N) return;
  float acc=0; for(int k=0;k<K;k++) acc+=A[m*K+k]*bf2f(B[k*N+n]);
  C[m*N+n]=acc;
}
__global__ void k_phi(const float* qkv, const u16* wphi, float* phiq, float* phik){
  int idx=blockIdx.x*blockDim.x+threadIdx.x; if(idx>=S*H*2) return;
  int w=idx&1; int h=(idx>>1)%H; int r=idx/(2*H);
  float p[16];
  #pragma unroll
  for(int f=0;f<16;f++){ float a=0; for(int d=0;d<16;d++) a+=qkv[r*1536+w*512+h*16+d]*bf2f(wphi[h*256+d*16+f]); p[f]=a; }
  float mx=-1e30f, mn=1e30f;
  #pragma unroll
  for(int f=0;f<16;f++){ mx=fmaxf(mx,p[f]); mn=fminf(mn,p[f]); }
  float s1=0,s2=0;
  #pragma unroll
  for(int f=0;f<16;f++){ s1+=__expf(p[f]-mx); s2+=__expf(mn-p[f]); }
  float* o=(w?phik:phiq)+(r*H+h)*32;
  #pragma unroll
  for(int f=0;f<16;f++){ o[f]=__expf(p[f]-mx)/s1; o[16+f]=__expf(mn-p[f])/s2; }
}
__global__ void k_kv(const float* phik, const float* qkv, float* kv){
  int idx=blockIdx.x*blockDim.x+threadIdx.x; if(idx>=H*32*17) return;
  int d=idx%17; int f=(idx/17)%32; int h=idx/(17*32);
  float a=0; for(int r=0;r<S;r++){ float p=phik[(r*H+h)*32+f]; a+= d<16? p*qkv[r*1536+1024+h*16+d] : p; }
  kv[idx]=a;
}
__global__ void k_o(const float* phiq, const float* kv, float* o){
  int idx=blockIdx.x*blockDim.x+threadIdx.x; if(idx>=S*D) return;
  int d=idx%16; int h=(idx/16)%H; int r=idx/D;
  float n=0,de=0; for(int f=0;f<32;f++){ float p=phiq[(r*H+h)*32+f]; n+=p*kv[(h*32+f)*17+d]; de+=p*kv[(h*32+f)*17+16]; }
  o[idx]=n/(de+1e-6f);
}
__global__ void k_res(const u16* x, const float* a, u16* h){ int i=blockIdx.x*blockDim.x+threadIdx.x; h[i]=f2bf(bf2f(x[i])+a[i]); }
__global__ void k_gelu(float* a, const u16* b1){ int i=blockIdx.x*blockDim.x+threadIdx.x; if(i>=S*F) return; float v=a[i]+bf2f(b1[i%F]); float t=tanhf(0.7978845608f*(v+0.044715f*v*v*v)); a[i]=0.5f*v*(1+t); }
__global__ void k_fin(const u16* h, const float* a, const u16* b2, u16* out){ int i=blockIdx.x*blockDim.x+threadIdx.x; out[i]=f2bf(bf2f(h[i])+a[i]+bf2f(b2[i%D])); }

extern "C" void kernel_launch(void* const* d_in, const int* in_sizes, int n_in, void* d_out, int out_size, void* d_ws, size_t ws_size, hipStream_t stream){
  const u16* x=(const u16*)d_in[0]; const u16* wqkv=(const u16*)d_in[1]; const u16* wphi=(const u16*)d_in[2]; const u16* wo=(const u16*)d_in[3];
  const u16* w1=(const u16*)d_in[5]; const u16* b1=(const u16*)d_in[6]; const u16* w2=(const u16*)d_in[7]; const u16* b2=(const u16*)d_in[8];
  float* ws=(float*)d_ws;
  float* ln=ws; float* qkv=ln+S*D; float* phiq=qkv+S*1536; float* phik=phiq+S*1024; float* kv=phik+S*1024; float* o=kv+32768; float* att=o+S*D; u16* h=(u16*)(att+S*D); float* hid=att+2*S*D; float* y=hid+S*F;
  k_ln<<<S,64,0,stream>>>(x,ln);
  k_gemm<<<dim3(1536/256,S),256,0,stream>>>(ln,wqkv,qkv,S,1536,D);
  k_phi<<<S*H*2/256,256,0,stream>>>(qkv,wphi,phiq,phik);
  k_kv<<<(H*32*17+63)/64,64,0,stream>>>(phik,qkv,kv);
  k_o<<<S*D/256,256,0,stream>>>(phiq,kv,o);
  k_gemm<<<dim3(2,S),256,0,stream>>>(o,wo,att,S,D,D);
  k_res<<<S*D/256,256,0,stream>>>(x,att,h);
  k_ln<<<S,64,0,stream>>>(h,ln);
  k_gemm<<<dim3((F+255)/256,S),256,0,stream>>>(ln,w1,hid,S,F,D);
  k_gelu<<<(S*F+255)/256,256,0,stream>>>(hid,b1);
  k_gemm<<<dim3(2,S),256,0,stream>>>(hid,w2,y,S,D,F);
  k_fin<<<S*D/256,256,0,stream>>>(h,y,b2,(u16*)d_out);
}
```

```cpp
#include <hip/hip_runtime.h>
#include <hip/hip_bf16.h>

typedef unsigned short u16;
typedef unsigned u32;
typedef unsigned long long u64;
typedef u32 v4u __attribute__((ext_vector_type(4)));
typedef u32 v2u __attribute__((ext_vector_type(2)));
typedef float f32x2 __attribute__((ext_vector_type(2)));
typedef float f32x4 __attribute__((ext_vector_type(4)));
typedef float f32x16 __attribute__((ext_vector_type(16)));
typedef __bf16 bf16x2 __attribute__((ext_vector_type(2)));
typedef __bf16 bf16x8 __attribute__((ext_vector_type(8)));
typedef short s16x4 __attribute__((ext_vector_type(4)));
typedef __amdgpu_buffer_rsrc_t rsrc_t;
#define DI __device__ __forceinline__
#define LAS __attribute__((address_space(3)))
#define CAS __attribute__((address_space(4)))
#define UNR _Pragma("unroll")
#define SB() __builtin_amdgcn_sched_barrier(0)

constexpr int OFF_E = 0, OFF_F1 = 4096, OFF_F2A = OFF_F1 + 32768, OFF_F2B = OFF_F2A + 32768, OFF_KVP = OFF_F2B + 32768;
constexpr int OFF_O = 1 << 20, OFF_H = 3 << 20, OFF_HID = 5 << 20, WS_BYTES = 11 << 20;
constexpr int OFF_STAMP = 12 << 20;
constexpr int L_WK = 0, L_WV = 32768, L_WQ = 65536, L_XR = 98304;
constexpr int L_WO = 0, L_XRES = 151552, L_KVZ = 159744, L_RED = 0, L_AUG = 61440;
constexpr int LDS_BYTES = 163840;
constexpr int w1_base(int j) { return j == 0 ? 98304 : j == 1 ? 131072 : 65536; }
constexpr int w2_base(int p) { return p < 15 ? p * 4096 : p < 31 ? 98304 + (p - 15) * 4096 : p < 40 ? 61440 + (p - 31) * 4096 : (p - 40) * 4096; }
constexpr int ubase(int m) { return m < 11 ? 3 * m : 2 * m + 11; }
constexpr int SC1 = 16;
constexpr u32 SPIN_CAP = 1u << 16;

DI f32x16 mfma32(v4u a, v4u b, f32x16 c) { return __builtin_amdgcn_mfma_f32_32x32x16_bf16(__builtin_bit_cast(bf16x8, a), __builtin_bit_cast(bf16x8, b), c, 0, 0, 0); }
DI f32x4 mfma16(v4u a, v4u b, f32x4 c) { return __builtin_amdgcn_mfma_f32_16x16x32_bf16(__builtin_bit_cast(bf16x8, a), __builtin_bit_cast(bf16x8, b), c, 0, 0, 0); }
DI u32 pk(float a, float b) { f32x2 f = {a, b}; return __builtin_bit_cast(u32, __builtin_convertvector(f, bf16x2)); }
DI u32 pk2(f32x2 f) { return __builtin_bit_cast(u32, __builtin_convertvector(f, bf16x2)); }
DI float lo(u32 x) { return __builtin_bit_cast(float, x << 16); }
DI float hi(u32 x) { return __builtin_bit_cast(float, x & 0xffff0000u); }
DI float dot2(u32 a, u32 b, float c) { return __builtin_amdgcn_fdot2_f32_bf16(__builtin_bit_cast(bf16x2, a), __builtin_bit_cast(bf16x2, b), c, false); }
constexpr u32 ONES = 0x3f803f80u;
DI v2u tr8(LAS char* p) { return __builtin_bit_cast(v2u, __builtin_amdgcn_ds_read_tr16_b64_v4i16((LAS s16x4*)p)); }
DI v4u trfrag(LAS char* p, int second) { v2u a = tr8(p), b = tr8(p + second); return (v4u){a.x, a.y, b.x, b.y}; }
DI v4u lds128(LAS char* p) { return *(LAS v4u*)p; }
DI void bar() { SB(); asm volatile("s_waitcnt lgkmcnt(0)\n\ts_barrier" ::: "memory"); SB(); }
#define PIN(x) asm volatile("" : "+v"(x))
template <int N> DI void vmwait() { asm volatile("s_waitcnt vmcnt(%0)" ::"i"(N) : "memory"); }
DI void glds(u32 lds_addr, u32 voff, rsrc_t rs, u32 soff) {
  asm volatile("s_mov_b32 m0, %0\n\ts_nop 0\n\tbuffer_load_dwordx4 %1, %2, %3 offen lds" ::"s"(lds_addr), "v"(voff), "s"(rs), "s"(soff) : "memory");
}
DI v4u ldg(rsrc_t rs, u32 voff, u32 soff) { return __builtin_amdgcn_raw_buffer_load_b128(rs, voff, soff, SC1); }
DI void stg(v4u v, rsrc_t rs, u32 voff, u32 soff) { __builtin_amdgcn_raw_buffer_store_b128(v, rs, voff + soff, 0, SC1); }
DI u32 umax3(u32 a, u32 b, u32 c) { return max(max(a, b), c); }
DI u32 mx4(u32 m, v4u d) { return umax3(umax3(m, d.x, d.y), d.z, d.w); }
DI bool anybad(u32 m) { return __builtin_amdgcn_ballot_w64(m == 0xffffffffu) != 0ull; }
DI float fbits(u32 x) { return __builtin_bit_cast(float, x); }
DI u32 ubits(float x) { return __builtin_bit_cast(u32, x); }
DI float xadd32(float a) { auto r = __builtin_amdgcn_permlane32_swap(ubits(a), ubits(a), false, false); return fbits(r[0]) + fbits(r[1]); }
DI float xmax32(float a) { auto r = __builtin_amdgcn_permlane32_swap(ubits(a), ubits(a), false, false); return __builtin_fmaxf(fbits(r[0]), fbits(r[1])); }
DI float xmin32(float a) { auto r = __builtin_amdgcn_permlane32_swap(ubits(a), ubits(a), false, false); return __builtin_fminf(fbits(r[0]), fbits(r[1])); }
DI float xadd16(float a) { auto r = __builtin_amdgcn_permlane16_swap(ubits(a), ubits(a), false, false); return fbits(r[0]) + fbits(r[1]); }
template <int CTRL> DI float dppadd(float a) { return a + fbits(__builtin_amdgcn_update_dpp(0u, ubits(a), CTRL, 0xf, 0xf, true)); }
DI float rowsum16(float a) { a = dppadd<0x128>(a); a = dppadd<0x124>(a); a = dppadd<0x122>(a); return dppadd<0x121>(a); }
DI v4u pack8(const f32x16& a, int o) { return (v4u){pk(a[o], a[o + 1]), pk(a[o + 2], a[o + 3]), pk(a[o + 4], a[o + 5]), pk(a[o + 6], a[o + 7])}; }
DI f32x16 zero16() { f32x16 z; UNR for (int i = 0; i < 16; ++i) z[i] = 0.f; return z; }
DI v4u aug_a(float cs, u32 bias_bits) {
  const u32 h2 = pk(cs, cs); const float ch = lo(h2);
  const u32 l2 = pk(cs - ch, 0.f);
  return (v4u){(h2 & 0xffffu) | (l2 << 16), (h2 & 0xffffu) | (bias_bits << 16), bias_bits, 0u};
}
DI v4u aug_b(float mean, float sd) {
  const u32 h2 = pk(-mean, -mean); const u32 s2 = pk(sd, sd);
  const u32 l2 = pk(-mean - lo(h2), sd - lo(s2));
  return (v4u){h2, (l2 & 0xffffu) | (s2 << 16), l2 >> 16, 0u};
}
DI void dual_softmax(const f32x16& p, int o, float c, v4u& pos, v4u& neg) {
  float mx = __builtin_fmaxf(__builtin_fmaxf(__builtin_fmaxf(p[o], p[o + 1]), __builtin_fmaxf(p[o + 2], p[o + 3])), __builtin_fmaxf(__builtin_fmaxf(p[o + 4], p[o + 5]), __builtin_fmaxf(p[o + 6], p[o + 7])));
  float mn = __builtin_fminf(__builtin_fminf(__builtin_fminf(p[o], p[o + 1]), __builtin_fminf(p[o + 2], p[o + 3])), __builtin_fminf(__builtin_fminf(p[o + 4], p[o + 5]), __builtin_fminf(p[o + 6], p[o + 7])));
  mx = xmax32(mx); mn = xmin32(mn);
  const float cmx = -c * mx, cmn = c * mn;
  f32x2 ep[4], en[4];
  UNR for (int j = 0; j < 4; ++j) {
    const f32x2 v = {p[o + 2 * j], p[o + 2 * j + 1]};
    const f32x2 a = v * c + cmx, b = v * (-c) + cmn;
    ep[j] = (f32x2){__builtin_amdgcn_exp2f(a.x), __builtin_amdgcn_exp2f(a.y)};
    en[j] = (f32x2){__builtin_amdgcn_exp2f(b.x), __builtin_amdgcn_exp2f(b.y)};
  }
  const f32x2 sp2 = (ep[0] + ep[1]) + (ep[2] + ep[3]), sn2 = (en[0] + en[1]) + (en[2] + en[3]);
  const float rp = __builtin_amdgcn_rcpf(xadd32(sp2.x + sp2.y)), rn = __builtin_amdgcn_rcpf(xadd32(sn2.x + sn2.y));
  pos = (v4u){pk2(ep[0] * rp), pk2(ep[1] * rp), pk2(ep[2] * rp), pk2(ep[3] * rp)};
  neg = (v4u){pk2(en[0] * rn), pk2(en[1] * rn), pk2(en[2] * rn), pk2(en[3] * rn)};
}
DI v4u gelu8(const f32x16& a, int o, float c1, float c2, float sd) {
  u32 r[4];
  UNR for (int j = 0; j < 4; ++j) {
    const f32x2 v = {a[o + 2 * j], a[o + 2 * j + 1]};
    const f32x2 g = (v * v * c2 + c1) * v;
    const f32x2 e = {__builtin_amdgcn_exp2f(g.x), __builtin_amdgcn_exp2f(g.y)};
    const f32x2 d = e * sd + sd;
    const f32x2 q = {__builtin_amdgcn_rcpf(d.x), __builtin_amdgcn_rcpf(d.y)};
    r[j] = pk2(v * q);
  }
  return (v4u){r[0], r[1], r[2], r[3]};
}

#define ST(i)
#define STN(i, v)
#define ST_OUT()

struct Params {
  const u16 *x, *wqkv, *wphi, *wo, *w1, *b1, *w2, *b2;
  u16* out;
  char* ws;
};

template <int NF, int W, int G> DI void load_group(v4u (&f)[W * 4], rsrc_t rs, u32 voff, u32 soff) {
  UNR for (int i = 0; i < 4; ++i) if (G * 4 + i < NF) f[(G % W) * 4 + i] = ldg(rs, voff + ((G * 4 + i) & 3) * 1024, soff + (G * 4 + i) / 4 * 4096);
}
template <int NF, int W, int G> DI bool group_bad(v4u (&f)[W * 4]) {
  u32 m = 0;
  UNR for (int i = 0; i < 4; ++i) if (G * 4 + i < NF) m = mx4(m, f[(G % W) * 4 + i]);
  return anybad(m);
}
template <int NF, int W, int G, int LAST> DI void load_range(v4u (&f)[W * 4], rsrc_t rs, u32 voff, u32 soff) {
  if constexpr (G <= LAST && G * 4 < NF) { load_group<NF, W, G>(f, rs, voff, soff); load_range<NF, W, G + 1, LAST>(f, rs, voff, soff); }
}
template <int NF, int W, int G, int LAST> DI void ensure_group(v4u (&f)[W * 4], rsrc_t rs, u32 voff, u32 soff, u32& nretry) {
  if (__builtin_expect(group_bad<NF, W, G>(f), 0)) {
    u32 spins = 0;
    do { asm volatile("" ::: "memory"); load_group<NF, W, G>(f, rs, voff, soff); ++spins; } while (group_bad<NF, W, G>(f) && spins < SPIN_CAP);
    nretry += spins;
    load_range<NF, W, G + 1, LAST>(f, rs, voff, soff);
    SB();
  }
}
template <int I, int N, class F> DI void sfor(F&& f) { if constexpr (I < N) { f(std::integral_constant<int, I>{}); sfor<I + 1, N>(f); } }

__global__ void __launch_bounds__(512, 2) fused_block(Params p) {
  __shared__ __attribute__((aligned(1024))) char smem[LDS_BYTES];
  LAS char* L = (LAS char*)smem;
  const int lane = threadIdx.x & 63, wv = __builtin_amdgcn_readfirstlane(threadIdx.x >> 6), w = wv & 3;
  const int b = blockIdx.x, xc = b & 7, ib = b >> 3;
  const int rb1 = (xc & 1) * 8 + (ib & 7), hp = (xc >> 1) * 4 + (ib >> 3);
  const int rb = xc * 2 + (ib >> 4), m = ib & 15, th = m & 1, cg = m >> 1;
  const int u0 = m < 11 ? 3 * m : 2 * m + 11;
  const bool three = m < 11;
  const int tt = w & 1, kh = w >> 1;
  const int h = lane >> 5, t = lane & 31;
  const rsrc_t rws = __builtin_amdgcn_make_buffer_rsrc(p.ws, 0, WS_BYTES, 0x00020000);
  const u64 tag = *(const CAS u64*)(p.ws + OFF_E) + 0x9E3779B97F4A7C15ull;
  const v4u tagv = {(u32)tag, (u32)(tag >> 32), (u32)tag, (u32)(tag >> 32)};
  const v4u sent = {0xffffffffu, 0xffffffffu, 0xffffffffu, 0xffffffffu};
  const u32 l16 = lane * 16;
  const u32 o_off = OFF_O + ((rb1 * 4 + w) * 32 + hp * 2) * 1024;
  const u32 h_off = OFF_H + ((rb * 4 + th * 2 + tt) * 32 + cg * 4 + kh * 2) * 1024;
  const u32 hid_off = OFF_HID + ((rb * 4 + w) * 86 + m) * 1024;
  const u32 kvp_base = OFF_KVP + (hp * 4 + w) * 16 * 640;
  ST(0);

  if (wv >= 4) {
    const u32 lb = (u32)(size_t)L;
    const rsrc_t rx = __builtin_amdgcn_make_buffer_rsrc((void*)p.x, 0, 2048 * 512 * 2, 0x00020000);
    const rsrc_t rq = __builtin_amdgcn_make_buffer_rsrc((void*)p.wqkv, 0, 512 * 1536 * 2, 0x00020000);
    const rsrc_t rp = __builtin_amdgcn_make_buffer_rsrc((void*)p.wphi, 0, 32 * 256 * 2, 0x00020000);
    const u32 vx0 = (lane >> 3) * 1024 + (((lane & 7) ^ (lane >> 4)) * 16), vx1 = vx0 ^ 64;
    const u32 vq = (lane >> 2) * 3072 + (lane & 3) * 16;
    const int rot = (rb1 + hp) & 7;
    if (lane < 16) glds(lb + L_WQ + w * 256, l16, rp, hp * 1024 + w * 256);
    auto chunk = [&](int c) {
      const int kc = (c + rot) & 7;
      const u32 sq = (kc * 64 + w * 16) * 3072 + hp * 64;
      glds(lb + L_WK + (c * 4 + w) * 1024, vq, rq, sq + 1024);
      glds(lb + L_WV + (c * 4 + w) * 1024, vq, rq, sq + 2048);
      UNR for (int j = 0; j < 4; ++j) glds(lb + L_XR + (c & 3) * 16384 + w * 4096 + j * 1024, (j & 1) ? vx1 : vx0, rx, (rb1 * 128 + w * 32 + j * 8) * 1024 + kc * 128);
    };
    chunk(0); chunk(1); chunk(2); chunk(3);
    vmwait<18>(); bar();
    UNR for (int c = 1; c < 5; ++c) { vmwait<12>(); bar(); chunk(c + 3); }
    vmwait<12>(); bar();
    UNR for (int c = 0; c < 8; ++c) glds(lb + L_WQ + (c * 4 + w) * 1024, vq, rq, ((((c + rot) & 7) * 64 + w * 16) * 3072 + hp * 64));
    vmwait<14>(); bar();
    vmwait<8>(); bar();
    ST(1);
    stg(sent, rws, l16, o_off); stg(sent, rws, l16, o_off + 1024);
    stg(sent, rws, l16, h_off); stg(sent, rws, l16, h_off + 1024);
    UNR for (int j = 0; j < 3; ++j) if (j < 2 || three) { stg(sent, rws, l16, hid_off + j * 16384); stg(sent, rws, l16, hid_off + j * 16384 + 43 * 1024); }
    {
      u32 spins = 0; bool bad;
      do {
        asm volatile("" ::: "memory");
        const v2u f = __builtin_amdgcn_raw_buffer_load_b64(rws, (lane & 15) * 128, OFF_F1 + hp * 2048, SC1);
        bad = __builtin_amdgcn_ballot_w64(f.x != tagv.x || f.y != tagv.y) != 0ull;
      } while (bad && ++spins < SPIN_CAP);
      STN(40, spins);
    }
    vmwait<0>();
    ST(2);
    bar();
    if (w == 0 && lane < 16) stg(tagv, rws, (lane & 7) * 16 + (lane < 8 ? OFF_F2A + (rb1 * 16 + hp) * 128 : OFF_F2B + (rb * 16 + m) * 128), 0);
    const rsrc_t ro = __builtin_amdgcn_make_buffer_rsrc((void*)p.wo, 0, 512 * 512 * 2, 0x00020000);
    const u32 vo = (lane >> 3) * 1024 + (((lane & 7) ^ ((lane >> 2) & 4)) * 16);
    UNR for (int i = 0; i < 16; ++i) { const int ii = ((i + rb * 2 + th) & 15) * 4 + w; glds(lb + L_WO + ii * 1024, vo, ro, ii * 8192 + cg * 128); }
    bar();
    ST(3);
    UNR for (int j = 0; j < 2; ++j) glds(lb + L_XRES + w * 2048 + j * 1024, j ? vx1 : vx0, rx, (rb * 128 + th * 64 + w * 16 + j * 8) * 1024 + cg * 128);
    {
      v4u d[8], z; u32 spins = 0; bool bad;
      const u32 vg = ((lane >> 4) & 1) * 640 + (h * 16 + (lane & 15)) * 16, vz = (lane & 15) * 640 + 512 + h * 64;
      const u32 vf = (lane & 15) * 128 + rb * 2048 + ((lane & 16) ? OFF_F2A : OFF_F2B);
      do {
        asm volatile("" ::: "memory");
        UNR for (int i = 0; i < 8; ++i) d[i] = ldg(rws, vg, kvp_base + i * 1280);
        z = ldg(rws, vz, kvp_base);
        const v2u f = __builtin_amdgcn_raw_buffer_load_b64(rws, vf, 0, SC1);
        u32 mm = mx4(0, z);
        UNR for (int i = 0; i < 8; ++i) mm = mx4(mm, d[i]);
        bad = __builtin_amdgcn_ballot_w64(mm == 0xffffffffu || f.x != tagv.x || f.y != tagv.y) != 0ull;
      } while (bad && ++spins < SPIN_CAP);
      STN(41, spins);
      ST(4);
      f32x2 s[4];
      UNR for (int k = 0; k < 4; ++k) {
        s[k] = (f32x2){lo(d[0][k]), hi(d[0][k])};
        UNR for (int i = 1; i < 8; ++i) s[k] += (f32x2){lo(d[i][k]), hi(d[i][k])};
      }
      v4u o;
      UNR for (int k = 0; k < 4; ++k) {
        const float a0 = xadd16(s[k].x), a1 = xadd16(s[k].y), z0 = rowsum16(lo(z[k])), z1 = rowsum16(hi(z[k]));
        o[k] = (lane & 16) ? pk(z0, z1) : pk(a0, a1);
      }
      *(LAS v4u*)(L + L_KVZ + w * 1024 + l16) = o;
    }
    vmwait<0>();
    bar();
    ST(5);
    const rsrc_t r1 = __builtin_amdgcn_make_buffer_rsrc((void*)p.w1, 0, 512 * 1376 * 2, 0x00020000);
    const u32 v1 = (lane >> 2) * 2752 + (lane & 3) * 16;
    UNR for (int i = 0; i < 8; ++i) {
      const int ks = i * 4 + w; const u32 s1 = ks * 16 * 2752 + u0 * 64;
      glds(lb + w1_base(0) + ks * 1024, v1, r1, s1);
      if (i < 5) glds(lb + w1_base(1) + ks * 1024, v1, r1, s1 + 64);
      if (three) glds(lb + w1_base(2) + ks * 1024, v1, r1, s1 + 128);
    }
    ST(6);
    bar();
    UNR for (int i = 5; i < 8; ++i) { const int ks = i * 4 + w; glds(lb + w1_base(1) + ks * 1024, v1, r1, ks * 16 * 2752 + u0 * 64 + 64); }
    bar();
    bar();
    const rsrc_t r2 = __builtin_amdgcn_make_buffer_rsrc((void*)p.w2, 0, 1376 * 512 * 2, 0x00020000);
    const u32 s2 = w * 8192 + cg * 128;
    UNR for (int q = 0; q < 15; ++q) glds(lb + w2_base(q) + w * 1024, vo, r2, s2 + (ubase(q & 15) + (q >> 4)) * 32768);
    vmwait<15>();
    ST(7);
    bar();
    if (w < 2 || (w == 2 && three)) {
      const u32 bb = h ? 0u : (u32)p.b1[(u0 + w) * 32 + t];
      LAS char* wp = L + (w == 0 ? w1_base(0) : w == 1 ? w1_base(1) : w1_base(2)) + h * 512 + ((lane & 15) >> 2) * 64 + ((lane >> 4) & 1) * 32 + (lane & 3) * 8;
      float c0 = 0.f, c1 = 0.f;
#pragma unroll 8
      for (int ks = 0; ks < 32; ++ks) {
        const v4u f = trfrag(wp + ks * 1024, 256);
        c0 = dot2(f.x, ONES, c0); c1 = dot2(f.y, ONES, c1); c0 = dot2(f.z, ONES, c0); c1 = dot2(f.w, ONES, c1);
      }
      *(LAS v4u*)(L + L_AUG + w * 1024 + l16) = aug_a(c0 + c1, bb);
    }
    vmwait<0>();
    ST(8);
    bar();
    UNR for (int q = 15; q < 31; ++q) glds(lb + w2_base(q) + w * 1024, vo, r2, s2 + (ubase(q & 15) + (q >> 4)) * 32768);
    ST(9);
    bar();
    UNR for (int q = 31; q < 43; ++q) glds(lb + w2_base(q) + w * 1024, vo, r2, s2 + (ubase(q & 15) + (q >> 4)) * 32768);
    vmwait<12>();
    ST(10);
    bar();
    vmwait<0>();
    ST(11);
    bar();
    bar();
    bar();
    ST_OUT();
    return;
  }

  u32 nretry = 0;
  if (lane < 40) stg(sent, rws, l16, kvp_base + rb1 * 640);
  vmwait<0>();
  const int q4 = (lane & 15) >> 2, lsub = ((lane >> 4) & 1) * 32 + (lane & 3) * 8;
  LAS char* const wnat = L + h * 512 + q4 * 64 + lsub;
  LAS char* const wperm = L + h * 256 + q4 * 64 + lsub;
  v4u xf[32];
  v4u wp0, wp1;
  float rstd, mean;
  v4u baug;
  {
    f32x16 ak = zero16(), av = zero16();
    float ss0 = 0.f, ss1 = 0.f, sq0 = 0.f, sq1 = 0.f, ck = 0.f, cv = 0.f;
    LAS char* xa[4];
    UNR for (int ks = 0; ks < 4; ++ks) xa[ks] = L + L_XR + w * 4096 + t * 128 + (((2 * ks + h) ^ ((t >> 1) & 7)) * 16);
    UNR for (int c = 0; c < 8; ++c) {
      bar();
      if (c == 0) {
        ST(1);
        if (w == 0 && lane < 8) stg(tagv, rws, l16, OFF_F1 + (hp * 16 + rb1) * 128);
        const v4u f = trfrag(L + L_WQ + ((lane >> 4) & 1) * 512 + (h * 4 + q4) * 32 + (lane & 3) * 8, 256);
        UNR for (int k = 0; k < 4; ++k) { wp0[k] = (lane & 16) ? 0u : f[k]; wp1[k] = (lane & 16) ? f[k] : 0u; }
      }
      if (c == 7) ST(2);
      UNR for (int ks = 0; ks < 4; ++ks) {
        const v4u x = lds128(xa[ks] + (c & 3) * 16384);
        xf[c * 4 + ks] = x;
        const v4u fk = trfrag(wnat + L_WK + (c * 4 + ks) * 1024, 256), fv = trfrag(wnat + L_WV + (c * 4 + ks) * 1024, 256);
        ak = mfma32(fk, x, ak); av = mfma32(fv, x, av);
        ss0 = dot2(x.x, ONES, ss0); ss1 = dot2(x.y, ONES, ss1); ss0 = dot2(x.z, ONES, ss0); ss1 = dot2(x.w, ONES, ss1);
        sq0 = dot2(x.x, x.x, sq0); sq1 = dot2(x.y, x.y, sq1); sq0 = dot2(x.z, x.z, sq0); sq1 = dot2(x.w, x.w, sq1);
        UNR for (int k = 0; k < 4; ++k) { ck = dot2(fk[k], ONES, ck); cv = dot2(fv[k], ONES, cv); }
      }
      PIN(ss0); PIN(ss1); PIN(sq0); PIN(sq1); PIN(ck); PIN(cv);
    }
    mean = xadd32(ss0 + ss1) * (1.f / 512.f);
    const float var = __builtin_fmaxf(xadd32(sq0 + sq1) * (1.f / 512.f) - mean * mean, 0.f);
    rstd = __builtin_amdgcn_rsqf(var + 1e-5f);
    baug = aug_b(mean, 0.f);
    ak = mfma32(aug_a(ck, 0u), baug, ak);
    av = mfma32(aug_a(cv, 0u), baug, av);
    f32x16 pp = mfma32(wp0, pack8(ak, 0), zero16());
    pp = mfma32(wp1, pack8(ak, 8), pp);
    LAS char* img = L + L_XR + w * 4096 + t * 32 + h * 16;
    av *= rstd;
    *(LAS v4u*)(img + 16384) = pack8(av, 0);
    *(LAS v4u*)(img + 16384 + 1024) = pack8(av, 8);
    UNR for (int e = 0; e < 2; ++e) {
      v4u pos, neg;
      dual_softmax(pp, 8 * e, rstd * 1.44269504f, pos, neg);
      *(LAS v4u*)(img + e * 2048) = pos;
      *(LAS v4u*)(img + e * 2048 + 1024) = neg;
    }
  }
  ST(3);
  bar();
  ST(4);
  {
    f32x4 kv = {0.f, 0.f, 0.f, 0.f}, zz = {0.f, 0.f, 0.f, 0.f};
    const v4u ones = {ONES, ONES, ONES, ONES};
    UNR for (int ww = 0; ww < 4; ++ww) {
      const v4u a = trfrag(L + L_XR + ww * 4096 + w * 1024 + lane * 8, 512);
      const v4u v = trfrag(L + L_XR + 16384 + ww * 4096 + (w >> 1) * 1024 + lane * 8, 512);
      kv = mfma16(a, v, kv); zz = mfma16(a, ones, zz);
    }
    const auto s0 = __builtin_amdgcn_permlane16_swap(pk(kv[0], kv[1]), pk(zz[0], zz[1]), false, false);
    const auto s1 = __builtin_amdgcn_permlane16_swap(pk(kv[2], kv[3]), pk(zz[2], zz[3]), false, false);
    const v4u o = {s0[0], s1[0], s0[1], s1[1]};
    if (t < 20) stg(o, rws, t < 16 ? (h * 16 + t) * 16 : 512 + (h * 4 + t - 16) * 16, kvp_base + rb1 * 640);
  }
  bar();
  ST(5);
  v4u phq[4];
  {
    f32x16 aq = zero16();
    float c0 = 0.f, c1 = 0.f;
    UNR for (int ks = 0; ks < 32; ++ks) {
      const v4u f = trfrag(wnat + L_WQ + ks * 1024, 256);
      aq = mfma32(f, xf[ks], aq);
      c0 = dot2(f.x, ONES, c0); c1 = dot2(f.y, ONES, c1); c0 = dot2(f.z, ONES, c0); c1 = dot2(f.w, ONES, c1);
    }
    aq = mfma32(aug_a(c0 + c1, 0u), baug, aq);
    f32x16 pp = mfma32(wp0, pack8(aq, 0), zero16());
    pp = mfma32(wp1, pack8(aq, 8), pp);
    dual_softmax(pp, 0, rstd * 1.44269504f, phq[0], phq[1]);
    dual_softmax(pp, 8, rstd * 1.44269504f, phq[2], phq[3]);
    PIN(phq[0]); PIN(phq[1]); PIN(phq[2]); PIN(phq[3]);
  }
  ST(6);
  bar();
  ST(7);
  UNR for (int e = 0; e < 2; ++e) {
    f32x16 a = mfma32(lds128(L + L_KVZ + e * 2048 + l16), phq[2 * e], zero16());
    a = mfma32(lds128(L + L_KVZ + e * 2048 + 1024 + l16), phq[2 * e + 1], a);
    const float r = __builtin_amdgcn_rcpf(a[8] + 1e-6f);
    a *= r;
    stg(pack8(a, 0), rws, l16, o_off + e * 1024);
  }
  v4u hres[2];
  {
    f32x16 keep, send = zero16();
    UNR for (int g = 0; g < 4; ++g) {
      const v2u xr = *(LAS v2u*)(L + L_XRES + (tt * 32 + t) * 128 + (((kh * 4 + g) ^ ((t >> 1) & 7)) * 16) + h * 8);
      keep[4 * g] = lo(xr.x); keep[4 * g + 1] = hi(xr.x); keep[4 * g + 2] = lo(xr.y); keep[4 * g + 3] = hi(xr.y);
    }
    LAS char* wkeep = L + L_WO + kh * 32768 + h * 1024 + q4 * 128 + ((kh ^ (q4 >> 1)) * 64) + lsub;
    LAS char* wsend = L + L_WO + kh * 32768 + h * 1024 + q4 * 128 + ((kh ^ 1 ^ (q4 >> 1)) * 64) + lsub;
    const u32 so = OFF_O + ((rb * 4 + th * 2 + tt) * 32 + kh * 16) * 1024;
    v4u f[16];
    ST(8);
    vmwait<0>();
    ST(9);
    load_range<16, 4, 0, 3>(f, rws, l16, so);
    SB();
    sfor<0, 4>([&](auto G) {
      constexpr int g = G.value;
      ensure_group<16, 4, g, 3>(f, rws, l16, so, nretry);
      UNR for (int i = 0; i < 4; ++i) {
        keep = mfma32(trfrag(wkeep + (g * 4 + i) * 2048, 512), f[g * 4 + i], keep);
        send = mfma32(trfrag(wsend + (g * 4 + i) * 2048, 512), f[g * 4 + i], send);
      }
    });
    STN(40, nretry);
    ST(10);
    bar();
    UNR for (int g = 0; g < 4; ++g) *(LAS f32x4*)(L + L_RED + w * 4096 + g * 1024 + l16) = (f32x4){send[4 * g], send[4 * g + 1], send[4 * g + 2], send[4 * g + 3]};
    bar();
    UNR for (int g = 0; g < 4; ++g) {
      const f32x4 r = *(LAS f32x4*)(L + L_RED + (w ^ 2) * 4096 + g * 1024 + l16);
      UNR for (int k = 0; k < 4; ++k) keep[4 * g + k] += r[k];
    }
    hres[0] = pack8(keep, 0); hres[1] = pack8(keep, 8);
    stg(hres[0], rws, l16, h_off); stg(hres[1], rws, l16, h_off + 1024);
    bar();
  }
  v4u pre[6];
  LAS char* const w2keep = L + kh * 2048 + h * 512 + q4 * 128 + ((kh ^ (q4 >> 1)) * 64) + lsub;
  LAS char* const w2send = L + kh * 2048 + h * 512 + q4 * 128 + ((kh ^ 1 ^ (q4 >> 1)) * 64) + lsub;
  {
    const u32 so = OFF_H + (rb * 4 + w) * 32768;
    ST(11);
    vmwait<0>();
    ST(12);
    load_range<32, 8, 0, 3>(xf, rws, l16, so);
    SB();
    bar();
    f32x16 a0 = zero16(), a1 = zero16();
    float ss0 = 0.f, ss1 = 0.f, sq0 = 0.f, sq1 = 0.f;
    sfor<0, 8>([&](auto G) {
      constexpr int g = G.value;
      ensure_group<32, 8, g, g + 3>(xf, rws, l16, so, nretry);
      load_range<32, 8, g + 4, g + 4>(xf, rws, l16, so);
      SB();
      UNR for (int i = 0; i < 4; ++i) {
        const v4u x = xf[g * 4 + i];
        a0 = mfma32(trfrag(wperm + w1_base(0) + (g * 4 + i) * 1024, 512), x, a0);
        a1 = mfma32(trfrag(wperm + w1_base(1) + (g * 4 + i) * 1024, 512), x, a1);
        ss0 = dot2(x.x, ONES, ss0); ss1 = dot2(x.y, ONES, ss1); ss0 = dot2(x.z, ONES, ss0); ss1 = dot2(x.w, ONES, ss1);
        sq0 = dot2(x.x, x.x, sq0); sq1 = dot2(x.y, x.y, sq1); sq0 = dot2(x.z, x.z, sq0); sq1 = dot2(x.w, x.w, sq1);
      }
      PIN(ss0); PIN(ss1); PIN(sq0); PIN(sq1);
    });
    STN(41, nretry);
    const float mu = xadd32(ss0 + ss1) * (1.f / 512.f);
    const float ve = __builtin_fmaxf(xadd32(sq0 + sq1) * (1.f / 512.f) - mu * mu, 0.f) + 1e-5f;
    const float rs = __builtin_amdgcn_rsqf(ve), sd = ve * rs;
    const v4u ba = aug_b(mu, sd);
    const float c1 = -2.302208198f * rs, c2 = c1 * 0.044715f * rs * rs;
    ST(13);
    bar();
    ST(14);
    a0 = mfma32(lds128(L + L_AUG + l16), ba, a0);
    a1 = mfma32(lds128(L + L_AUG + 1024 + l16), ba, a1);
    if (three) {
      f32x16 a2 = mfma32(lds128(L + L_AUG + 2048 + l16), ba, zero16());
      UNR for (int ks = 0; ks < 32; ++ks) a2 = mfma32(trfrag(wperm + w1_base(2) + ks * 1024, 512), xf[ks], a2);
      stg(gelu8(a0, 0, c1, c2, sd), rws, l16, hid_off);
      stg(gelu8(a0, 8, c1, c2, sd), rws, l16, hid_off + 43 * 1024);
      UNR for (int q = 0; q < 3; ++q) { pre[2 * q] = trfrag(w2keep + w2_base(q), 1024); pre[2 * q + 1] = trfrag(w2send + w2_base(q), 1024); }
      ST(15);
      bar();
      stg(gelu8(a1, 0, c1, c2, sd), rws, l16, hid_off + 16384);
      stg(gelu8(a1, 8, c1, c2, sd), rws, l16, hid_off + 16384 + 43 * 1024);
      stg(gelu8(a2, 0, c1, c2, sd), rws, l16, hid_off + 32768);
      stg(gelu8(a2, 8, c1, c2, sd), rws, l16, hid_off + 32768 + 43 * 1024);
    } else {
      stg(gelu8(a0, 0, c1, c2, sd), rws, l16, hid_off);
      stg(gelu8(a0, 8, c1, c2, sd), rws, l16, hid_off + 43 * 1024);
      stg(gelu8(a1, 0, c1, c2, sd), rws, l16, hid_off + 16384);
      stg(gelu8(a1, 8, c1, c2, sd), rws, l16, hid_off + 16384 + 43 * 1024);
      UNR for (int q = 0; q < 3; ++q) { pre[2 * q] = trfrag(w2keep + w2_base(q), 1024); pre[2 * q + 1] = trfrag(w2send + w2_base(q), 1024); }
      ST(15);
      bar();
      vmwait<0>();
    }
  }
  {
    f32x16 keep, send = zero16();
    UNR for (int g = 0; g < 4; ++g) {
      const v2u bb = *(const v2u*)(p.b2 + cg * 64 + kh * 32 + g * 8 + h * 4);
      const u32 r0 = hres[g >> 1][(g & 1) * 2], r1 = hres[g >> 1][(g & 1) * 2 + 1];
      keep[4 * g] = lo(r0) + lo(bb.x); keep[4 * g + 1] = hi(r0) + hi(bb.x); keep[4 * g + 2] = lo(r1) + lo(bb.y); keep[4 * g + 3] = hi(r1) + hi(bb.y);
    }
    const u32 so = OFF_HID + ((rb * 4 + th * 2 + tt) * 86 + kh * 43) * 1024;
    v4u f[24];
    ST(16);
    load_range<43, 6, 0, 4>(f, rws, l16, so);
    SB();
    sfor<0, 11>([&](auto G) {
      constexpr int g = G.value;
      ensure_group<43, 6, g, g + 4>(f, rws, l16, so, nretry);
      load_range<43, 6, g + 5, g + 5>(f, rws, l16, so);
      SB();
      if (g == 0) ST(17);
      UNR for (int i = 0; i < 4; ++i) {
        const int q = g * 4 + i;
        if (q == 15 || q == 31) bar();
        if (q < 43) {
          keep = mfma32(q < 3 ? pre[2 * q] : trfrag(w2keep + w2_base(q), 1024), f[(g % 6) * 4 + i], keep);
          send = mfma32(q < 3 ? pre[2 * q + 1] : trfrag(w2send + w2_base(q), 1024), f[(g % 6) * 4 + i], send);
        }
      }
    });
    STN(42, nretry);
    ST(18);
    bar();
    UNR for (int g = 0; g < 4; ++g) *(LAS f32x4*)(L + 16384 + w * 4096 + g * 1024 + l16) = (f32x4){send[4 * g], send[4 * g + 1], send[4 * g + 2], send[4 * g + 3]};
    bar();
    UNR for (int g = 0; g < 4; ++g) {
      const f32x4 r = *(LAS f32x4*)(L + 16384 + (w ^ 2) * 4096 + g * 1024 + l16);
      UNR for (int k = 0; k < 4; ++k) keep[4 * g + k] += r[k];
    }
    u16* orow = p.out + (size_t)(rb * 128 + th * 64 + tt * 32 + t) * 512 + cg * 64 + kh * 32 + h * 8;
    UNR for (int g = 0; g < 2; ++g) {
      const v4u v = pack8(keep, 8 * g);
      const auto s0 = __builtin_amdgcn_permlane32_swap(v.x, v.z, false, false);
      const auto s1 = __builtin_amdgcn_permlane32_swap(v.y, v.w, false, false);
      *(v4u*)(orow + g * 16) = (v4u){s0[0], s1[0], s0[1], s1[1]};
    }
    if (b == 0 && threadIdx.x == 0) *(u64*)(p.ws + OFF_E) = tag;
  }
  ST(19);
  ST_OUT();
}

extern "C" void kernel_launch(void* const* d_in, const int* in_sizes, int n_in, void* d_out, int out_size, void* d_ws, size_t ws_size,
                              hipStream_t stream) {
  Params p;
  p.x = (const u16*)d_in[0]; p.wqkv = (const u16*)d_in[1]; p.wphi = (const u16*)d_in[2]; p.wo = (const u16*)d_in[3];
  p.w1 = (const u16*)d_in[5]; p.b1 = (const u16*)d_in[6]; p.w2 = (const u16*)d_in[7]; p.b2 = (const u16*)d_in[8];
  p.out = (u16*)d_out; p.ws = (char*)d_ws;
  fused_block<<<256, 512, 0, stream>>>(p);
}
```

```cpp
#include <hip/hip_runtime.h>
#include <hip/hip_bf16.h>

typedef unsigned short u16;
typedef unsigned u32;
typedef unsigned long long u64;
typedef u32 v4u __attribute__((ext_vector_type(4)));
typedef u32 v2u __attribute__((ext_vector_type(2)));
typedef float f32x2 __attribute__((ext_vector_type(2)));
typedef float f32x4 __attribute__((ext_vector_type(4)));
typedef float f32x16 __attribute__((ext_vector_type(16)));
typedef __bf16 bf16x2 __attribute__((ext_vector_type(2)));
typedef __bf16 bf16x8 __attribute__((ext_vector_type(8)));
typedef short s16x4 __attribute__((ext_vector_type(4)));
typedef __amdgpu_buffer_rsrc_t rsrc_t;
#define DI __device__ __forceinline__
#define LAS __attribute__((address_space(3)))
#define CAS __attribute__((address_space(4)))
#define UNR _Pragma("unroll")
#define SB() __builtin_amdgcn_sched_barrier(0)

constexpr int OFF_E = 0, OFF_F1 = 4096, OFF_F2A = OFF_F1 + 32768, OFF_F2B = OFF_F2A + 32768, OFF_KVP = OFF_F2B + 32768;
constexpr int OFF_O = 1 << 20, OFF_H = 3 << 20, OFF_HID = 5 << 20, WS_BYTES = 11 << 20;
constexpr int OFF_STAMP = 12 << 20;
constexpr int L_WK = 0, L_WV = 32768, L_WQ = 65536, L_XR = 98304;
constexpr int L_WO = 0, L_XRES = 151552, L_KVZ = 159744, L_RED = 0, L_AUG = 61440;
constexpr int LDS_BYTES = 163840;
constexpr int w1_base(int j) { return j == 0 ? 98304 : j == 1 ? 131072 : 65536; }
constexpr int w2_base(int p) { return p < 15 ? p * 4096 : p < 31 ? 98304 + (p - 15) * 4096 : p < 40 ? 61440 + (p - 31) * 4096 : (p - 40) * 4096; }
constexpr int ubase(int m) { return m < 11 ? 3 * m : 2 * m + 11; }
constexpr int SC1 = 16;
constexpr u32 SPIN_CAP = 1u << 16;

DI f32x16 mfma32(v4u a, v4u b, f32x16 c) { return __builtin_amdgcn_mfma_f32_32x32x16_bf16(__builtin_bit_cast(bf16x8, a), __builtin_bit_cast(bf16x8, b), c, 0, 0, 0); }
DI f32x4 mfma16(v4u a, v4u b, f32x4 c) { return __builtin_amdgcn_mfma_f32_16x16x32_bf16(__builtin_bit_cast(bf16x8, a), __builtin_bit_cast(bf16x8, b), c, 0, 0, 0); }
DI u32 pk(float a, float b) { f32x2 f = {a, b}; return __builtin_bit_cast(u32, __builtin_convertvector(f, bf16x2)); }
DI u32 pk2(f32x2 f) { return __builtin_bit_cast(u32, __builtin_convertvector(f, bf16x2)); }
DI float lo(u32 x) { return __builtin_bit_cast(float, x << 16); }
DI float hi(u32 x) { return __builtin_bit_cast(float, x & 0xffff0000u); }
DI float dot2(u32 a, u32 b, float c) { return __builtin_amdgcn_fdot2_f32_bf16(__builtin_bit_cast(bf16x2, a), __builtin_bit_cast(bf16x2, b), c, false); }
constexpr u32 ONES = 0x3f803f80u;
DI v2u tr8(LAS char* p) { return __builtin_bit_cast(v2u, __builtin_amdgcn_ds_read_tr16_b64_v4i16((LAS s16x4*)p)); }
DI v4u trfrag(LAS char* p, int second) { v2u a = tr8(p), b = tr8(p + second); return (v4u){a.x, a.y, b.x, b.y}; }
DI v4u lds128(LAS char* p) { return *(LAS v4u*)p; }
DI void bar() { SB(); asm volatile("s_waitcnt lgkmcnt(0)\n\ts_barrier" ::: "memory"); SB(); }
#define PIN(x) asm volatile("" : "+v"(x))
template <int N> DI void vmwait() { asm volatile("s_waitcnt vmcnt(%0)" ::"i"(N) : "memory"); }
DI void glds(u32 lds_addr, u32 voff, rsrc_t rs, u32 soff) {
  asm volatile("s_mov_b32 m0, %0\n\ts_nop 0\n\tbuffer_load_dwordx4 %1, %2, %3 offen lds" ::"s"(lds_addr), "v"(voff), "s"(rs), "s"(soff) : "memory");
}
DI v4u ldg(rsrc_t rs, u32 voff, u32 soff) { return __builtin_amdgcn_raw_buffer_load_b128(rs, voff, soff, SC1); }
DI void stg(v4u v, rsrc_t rs, u32 voff, u32 soff) { __builtin_amdgcn_raw_buffer_store_b128(v, rs, voff + soff, 0, SC1); }
DI u32 umax3(u32 a, u32 b, u32 c) { return max(max(a, b), c); }
DI u32 mx4(u32 m, v4u d) { return umax3(umax3(m, d.x, d.y), d.z, d.w); }
DI bool anybad(u32 m) { return __builtin_amdgcn_ballot_w64(m == 0xffffffffu) != 0ull; }
DI float fbits(u32 x) { return __builtin_bit_cast(float, x); }
DI u32 ubits(float x) { return __builtin_bit_cast(u32, x); }
DI float xadd32(float a) { auto r = __builtin_amdgcn_permlane32_swap(ubits(a), ubits(a), false, false); return fbits(r[0]) + fbits(r[1]); }
DI float xmax32(float a) { auto r = __builtin_amdgcn_permlane32_swap(ubits(a), ubits(a), false, false); return __builtin_fmaxf(fbits(r[0]), fbits(r[1])); }
DI float xmin32(float a) { auto r = __builtin_amdgcn_permlane32_swap(ubits(a), ubits(a), false, false); return __builtin_fminf(fbits(r[0]), fbits(r[1])); }
DI float xadd16(float a) { auto r = __builtin_amdgcn_permlane16_swap(ubits(a), ubits(a), false, false); return fbits(r[0]) + fbits(r[1]); }
template <int CTRL> DI float dppadd(float a) { return a + fbits(__builtin_amdgcn_update_dpp(0u, ubits(a), CTRL, 0xf, 0xf, true)); }
DI float rowsum16(float a) { a = dppadd<0x128>(a); a = dppadd<0x124>(a); a = dppadd<0x122>(a); return dppadd<0x121>(a); }
DI v4u pack8(const f32x16& a, int o) { return (v4u){pk(a[o], a[o + 1]), pk(a[o + 2], a[o + 3]), pk(a[o + 4], a[o + 5]), pk(a[o + 6], a[o + 7])}; }
DI f32x16 zero16() { f32x16 z; UNR for (int i = 0; i < 16; ++i) z[i] = 0.f; return z; }
DI v4u aug_a(float cs, u32 bias_bits) {
  const u32 h2 = pk(cs, cs); const float ch = lo(h2);
  const u32 l2 = pk(cs - ch, 0.f);
  return (v4u){(h2 & 0xffffu) | (l2 << 16), (h2 & 0xffffu) | (bias_bits << 16), bias_bits, 0u};
}
DI v4u aug_b(float mean, float sd) {
  const u32 h2 = pk(-mean, -mean); const u32 s2 = pk(sd, sd);
  const u32 l2 = pk(-mean - lo(h2), sd - lo(s2));
  return (v4u){h2, (l2 & 0xffffu) | (s2 << 16), l2 >> 16, 0u};
}
DI void dual_softmax(const f32x16& p, int o, float c, v4u& pos, v4u& neg) {
  float mx = __builtin_fmaxf(__builtin_fmaxf(__builtin_fmaxf(p[o], p[o + 1]), __builtin_fmaxf(p[o + 2], p[o + 3])), __builtin_fmaxf(__builtin_fmaxf(p[o + 4], p[o + 5]), __builtin_fmaxf(p[o + 6], p[o + 7])));
  float mn = __builtin_fminf(__builtin_fminf(__builtin_fminf(p[o], p[o + 1]), __builtin_fminf(p[o + 2], p[o + 3])), __builtin_fminf(__builtin_fminf(p[o + 4], p[o + 5]), __builtin_fminf(p[o + 6], p[o + 7])));
  mx = xmax32(mx); mn = xmin32(mn);
  const float cmx = -c * mx, cmn = c * mn;
  f32x2 ep[4], en[4];
  UNR for (int j = 0; j < 4; ++j) {
    const f32x2 v = {p[o + 2 * j], p[o + 2 * j + 1]};
    const f32x2 a = v * c + cmx, b = v * (-c) + cmn;
    ep[j] = (f32x2){__builtin_amdgcn_exp2f(a.x), __builtin_amdgcn_exp2f(a.y)};
    en[j] = (f32x2){__builtin_amdgcn_exp2f(b.x), __builtin_amdgcn_exp2f(b.y)};
  }
  const f32x2 sp2 = (ep[0] + ep[1]) + (ep[2] + ep[3]), sn2 = (en[0] + en[1]) + (en[2] + en[3]);
  const float rp = __builtin_amdgcn_rcpf(xadd32(sp2.x + sp2.y)), rn = __builtin_amdgcn_rcpf(xadd32(sn2.x + sn2.y));
  pos = (v4u){pk2(ep[0] * rp), pk2(ep[1] * rp), pk2(ep[2] * rp), pk2(ep[3] * rp)};
  neg = (v4u){pk2(en[0] * rn), pk2(en[1] * rn), pk2(en[2] * rn), pk2(en[3] * rn)};
}
DI v4u gelu8(const f32x16& a, int o, float c1, float c2, float sd) {
  u32 r[4];
  UNR for (int j = 0; j < 4; ++j) {
    const f32x2 v = {a[o + 2 * j], a[o + 2 * j + 1]};
    const f32x2 g = (v * v * c2 + c1) * v;
    const f32x2 e = {__builtin_amdgcn_exp2f(g.x), __builtin_amdgcn_exp2f(g.y)};
    const f32x2 d = e * sd + sd;
    const f32x2 q = {__builtin_amdgcn_rcpf(d.x), __builtin_amdgcn_rcpf(d.y)};
    r[j] = pk2(v * q);
  }
  return (v4u){r[0], r[1], r[2], r[3]};
}

#define ST(i)
#define STN(i, v)
#define ST_OUT()

#define SPIN_TO(us) do { while (__builtin_amdgcn_s_memrealtime() - t_start < (us) * 100ull) __builtin_amdgcn_s_sleep(1); } while (0)
struct Params {
  const u16 *x, *wqkv, *wphi, *wo, *w1, *b1, *w2, *b2;
  u16* out;
  char* ws;
};

template <int NF, int W, int G> DI void load_group(v4u (&f)[W * 4], rsrc_t rs, u32 voff, u32 soff) {
  UNR for (int i = 0; i < 4; ++i) if (G * 4 + i < NF) f[(G % W) * 4 + i] = ldg(rs, voff + ((G * 4 + i) & 3) * 1024, soff + (G * 4 + i) / 4 * 4096);
}
template <int NF, int W, int G> DI bool group_bad(v4u (&f)[W * 4]) {
  u32 m = 0;
  UNR for (int i = 0; i < 4; ++i) if (G * 4 + i < NF) m = mx4(m, f[(G % W) * 4 + i]);
  return anybad(m);
}
template <int NF, int W, int G, int LAST> DI void load_range(v4u (&f)[W * 4], rsrc_t rs, u32 voff, u32 soff) {
  if constexpr (G <= LAST && G * 4 < NF) { load_group<NF, W, G>(f, rs, voff, soff); load_range<NF, W, G + 1, LAST>(f, rs, voff, soff); }
}
template <int NF, int W, int G, int LAST> DI void ensure_group(v4u (&f)[W * 4], rsrc_t rs, u32 voff, u32 soff, u32& nretry) {
  if (__builtin_expect(group_bad<NF, W, G>(f), 0)) {
    u32 spins = 0;
    do { asm volatile("" ::: "memory"); load_group<NF, W, G>(f, rs, voff, soff); ++spins; } while (group_bad<NF, W, G>(f) && spins < SPIN_CAP);
    nretry += spins;
    load_range<NF, W, G + 1, LAST>(f, rs, voff, soff);
    SB();
  }
}
template <int I, int N, class F> DI void sfor(F&& f) { if constexpr (I < N) { f(std::integral_constant<int, I>{}); sfor<I + 1, N>(f); } }

__global__ void __launch_bounds__(512, 2) fused_block(Params p) {
  __shared__ __attribute__((aligned(1024))) char smem[LDS_BYTES];
  u64 t_start; asm volatile("s_memrealtime %0\n\ts_waitcnt lgkmcnt(0)" : "=s"(t_start)::"memory"); SB();
  LAS char* L = (LAS char*)smem;
  const int lane = threadIdx.x & 63, wv = __builtin_amdgcn_readfirstlane(threadIdx.x >> 6), w = wv & 3;
  const int b = blockIdx.x, xc = b & 7, ib = b >> 3;
  const int rb1 = (xc & 1) * 8 + (ib & 7), hp = (xc >> 1) * 4 + (ib >> 3);
  const int rb = xc * 2 + (ib >> 4), m = ib & 15, th = m & 1, cg = m >> 1;
  const int u0 = m < 11 ? 3 * m : 2 * m + 11;
  const bool three = m < 11;
  const int tt = w & 1, kh = w >> 1;
  const int h = lane >> 5, t = lane & 31;
  const rsrc_t rws = __builtin_amdgcn_make_buffer_rsrc(p.ws, 0, WS_BYTES, 0x00020000);
  const u64 tag = *(const CAS u64*)(p.ws + OFF_E) + 0x9E3779B97F4A7C15ull;
  const v4u tagv = {(u32)tag, (u32)(tag >> 32), (u32)tag, (u32)(tag >> 32)};
  const v4u sent = {0xffffffffu, 0xffffffffu, 0xffffffffu, 0xffffffffu};
  const u32 l16 = lane * 16;
  const u32 o_off = OFF_O + ((rb1 * 4 + w) * 32 + hp * 2) * 1024;
  const u32 h_off = OFF_H + ((rb * 4 + th * 2 + tt) * 32 + cg * 4 + kh * 2) * 1024;
  const u32 hid_off = OFF_HID + ((rb * 4 + w) * 86 + m) * 1024;
  const u32 kvp_base = OFF_KVP + (hp * 4 + w) * 16 * 640;

  if (wv >= 4) {
    const u32 lb = (u32)(size_t)L;
    const rsrc_t rx = __builtin_amdgcn_make_buffer_rsrc((void*)p.x, 0, 2048 * 512 * 2, 0x00020000);
    const rsrc_t rq = __builtin_amdgcn_make_buffer_rsrc((void*)p.wqkv, 0, 512 * 1536 * 2, 0x00020000);
    const rsrc_t rp = __builtin_amdgcn_make_buffer_rsrc((void*)p.wphi, 0, 32 * 256 * 2, 0x00020000);
    const u32 vx0 = (lane >> 3) * 1024 + (((lane & 7) ^ (lane >> 4)) * 16), vx1 = vx0 ^ 64;
    const u32 vq = (lane >> 2) * 3072 + (lane & 3) * 16;
    const int rot = (rb1 + hp) & 7;
    if (lane < 16) glds(lb + L_WQ + w * 256, l16, rp, hp * 1024 + w * 256);
    auto chunk = [&](int c) {
      const int kc = (c + rot) & 7;
      const u32 sq = (kc * 64 + w * 16) * 3072 + hp * 64;
      glds(lb + L_WK + (c * 4 + w) * 1024, vq, rq, sq + 1024);
      glds(lb + L_WV + (c * 4 + w) * 1024, vq, rq, sq + 2048);
      UNR for (int j = 0; j < 4; ++j) glds(lb + L_XR + (c & 3) * 16384 + w * 4096 + j * 1024, (j & 1) ? vx1 : vx0, rx, (rb1 * 128 + w * 32 + j * 8) * 1024 + kc * 128);
    };
    chunk(0); chunk(1); chunk(2); chunk(3);
    vmwait<18>(); bar();
    UNR for (int c = 1; c < 5; ++c) { vmwait<12>(); bar(); chunk(c + 3); }
    vmwait<12>(); bar();
    UNR for (int c = 0; c < 8; ++c) glds(lb + L_WQ + (c * 4 + w) * 1024, vq, rq, ((((c + rot) & 7) * 64 + w * 16) * 3072 + hp * 64));
    vmwait<14>(); bar();
    vmwait<8>(); bar();
    ST(1);
    stg(sent, rws, l16, o_off); stg(sent, rws, l16, o_off + 1024);
    stg(sent, rws, l16, h_off); stg(sent, rws, l16, h_off + 1024);
    UNR for (int j = 0; j < 3; ++j) if (j < 2 || three) { stg(sent, rws, l16, hid_off + j * 16384); stg(sent, rws, l16, hid_off + j * 16384 + 43 * 1024); }
    {
      u32 spins = 0; bool bad;
      do {
        asm volatile("" ::: "memory");
        const v2u f = __builtin_amdgcn_raw_buffer_load_b64(rws, (lane & 15) * 128, OFF_F1 + hp * 2048, SC1);
        bad = __builtin_amdgcn_ballot_w64(f.x != tagv.x || f.y != tagv.y) != 0ull;
      } while (bad && ++spins < SPIN_CAP);
      STN(40, spins);
    }
    vmwait<0>();
    ST(2);
    bar();
    if (w == 0 && lane < 16) stg(tagv, rws, (lane & 7) * 16 + (lane < 8 ? OFF_F2A + (rb1 * 16 + hp) * 128 : OFF_F2B + (rb * 16 + m) * 128), 0);
    const rsrc_t ro = __builtin_amdgcn_make_buffer_rsrc((void*)p.wo, 0, 512 * 512 * 2, 0x00020000);
    const u32 vo = (lane >> 3) * 1024 + (((lane & 7) ^ ((lane >> 2) & 4)) * 16);
    UNR for (int i = 0; i < 16; ++i) { const int ii = ((i + rb * 2 + th) & 15) * 4 + w; glds(lb + L_WO + ii * 1024, vo, ro, ii * 8192 + cg * 128); }
    bar();
    ST(3);
    UNR for (int j = 0; j < 2; ++j) glds(lb + L_XRES + w * 2048 + j * 1024, j ? vx1 : vx0, rx, (rb * 128 + th * 64 + w * 16 + j * 8) * 1024 + cg * 128);
    {
      v4u d[8], z; u32 spins = 0; bool bad;
      const u32 vg = ((lane >> 4) & 1) * 640 + (h * 16 + (lane & 15)) * 16, vz = (lane & 15) * 640 + 512 + h * 64;
      const u32 vf = (lane & 15) * 128 + rb * 2048 + ((lane & 16) ? OFF_F2A : OFF_F2B);
      do {
        asm volatile("" ::: "memory");
        UNR for (int i = 0; i < 8; ++i) d[i] = ldg(rws, vg, kvp_base + i * 1280);
        z = ldg(rws, vz, kvp_base);
        const v2u f = __builtin_amdgcn_raw_buffer_load_b64(rws, vf, 0, SC1);
        u32 mm = mx4(0, z);
        UNR for (int i = 0; i < 8; ++i) mm = mx4(mm, d[i]);
        bad = __builtin_amdgcn_ballot_w64(mm == 0xffffffffu || f.x != tagv.x || f.y != tagv.y) != 0ull;
      } while (bad && ++spins < SPIN_CAP);
      STN(41, spins);
      ST(4);
      f32x2 s[4];
      UNR for (int k = 0; k < 4; ++k) {
        s[k] = (f32x2){lo(d[0][k]), hi(d[0][k])};
        UNR for (int i = 1; i < 8; ++i) s[k] += (f32x2){lo(d[i][k]), hi(d[i][k])};
      }
      v4u o;
      UNR for (int k = 0; k < 4; ++k) {
        const float a0 = xadd16(s[k].x), a1 = xadd16(s[k].y), z0 = rowsum16(lo(z[k])), z1 = rowsum16(hi(z[k]));
        o[k] = (lane & 16) ? pk(z0, z1) : pk(a0, a1);
      }
      *(LAS v4u*)(L + L_KVZ + w * 1024 + l16) = o;
    }
    vmwait<0>();
    bar();
    ST(5);
    const rsrc_t r1 = __builtin_amdgcn_make_buffer_rsrc((void*)p.w1, 0, 512 * 1376 * 2, 0x00020000);
    const u32 v1 = (lane >> 2) * 2752 + (lane & 3) * 16;
    UNR for (int i = 0; i < 8; ++i) {
      const int ks = i * 4 + w; const u32 s1 = ks * 16 * 2752 + u0 * 64;
      glds(lb + w1_base(0) + ks * 1024, v1, r1, s1);
      if (i < 5) glds(lb + w1_base(1) + ks * 1024, v1, r1, s1 + 64);
      if (three) glds(lb + w1_base(2) + ks * 1024, v1, r1, s1 + 128);
    }
    ST(6);
    bar();
    UNR for (int i = 5; i < 8; ++i) { const int ks = i * 4 + w; glds(lb + w1_base(1) + ks * 1024, v1, r1, ks * 16 * 2752 + u0 * 64 + 64); }
    bar();
    bar();
    const rsrc_t r2 = __builtin_amdgcn_make_buffer_rsrc((void*)p.w2, 0, 1376 * 512 * 2, 0x00020000);
    const u32 s2 = w * 8192 + cg * 128;
    UNR for (int q = 0; q < 15; ++q) glds(lb + w2_base(q) + w * 1024, vo, r2, s2 + (ubase(q & 15) + (q >> 4)) * 32768);
    vmwait<15>();
    ST(7);
    bar();
    if (w < 2 || (w == 2 && three)) {
      const u32 bb = h ? 0u : (u32)p.b1[(u0 + w) * 32 + t];
      LAS char* wp = L + (w == 0 ? w1_base(0) : w == 1 ? w1_base(1) : w1_base(2)) + h * 512 + ((lane & 15) >> 2) * 64 + ((lane >> 4) & 1) * 32 + (lane & 3) * 8;
      float c0 = 0.f, c1 = 0.f;
#pragma unroll 8
      for (int ks = 0; ks < 32; ++ks) {
        const v4u f = trfrag(wp + ks * 1024, 256);
        c0 = dot2(f.x, ONES, c0); c1 = dot2(f.y, ONES, c1); c0 = dot2(f.z, ONES, c0); c1 = dot2(f.w, ONES, c1);
      }
      *(LAS v4u*)(L + L_AUG + w * 1024 + l16) = aug_a(c0 + c1, bb);
    }
    vmwait<0>();
    ST(8);
    bar();
    UNR for (int q = 15; q < 31; ++q) glds(lb + w2_base(q) + w * 1024, vo, r2, s2 + (ubase(q & 15) + (q >> 4)) * 32768);
    ST(9);
    bar();
    UNR for (int q = 31; q < 43; ++q) glds(lb + w2_base(q) + w * 1024, vo, r2, s2 + (ubase(q & 15) + (q >> 4)) * 32768);
    vmwait<12>();
    ST(10);
    bar();
    vmwait<0>();
    ST(11);
    bar();
    bar();
    bar();
    ST_OUT();
    return;
  }

  u32 nretry = 0;
  if (lane < 40) stg(sent, rws, l16, kvp_base + rb1 * 640);
  vmwait<0>();
  const int q4 = (lane & 15) >> 2, lsub = ((lane >> 4) & 1) * 32 + (lane & 3) * 8;
  LAS char* const wnat = L + h * 512 + q4 * 64 + lsub;
  LAS char* const wperm = L + h * 256 + q4 * 64 + lsub;
  v4u xf[32];
  v4u wp0, wp1;
  float rstd, mean;
  v4u baug;
  {
    f32x16 ak = zero16(), av = zero16();
    float ss0 = 0.f, ss1 = 0.f, sq0 = 0.f, sq1 = 0.f, ck = 0.f, cv = 0.f;
    LAS char* xa[4];
    UNR for (int ks = 0; ks < 4; ++ks) xa[ks] = L + L_XR + w * 4096 + t * 128 + (((2 * ks + h) ^ ((t >> 1) & 7)) * 16);
    UNR for (int c = 0; c < 8; ++c) {
      bar();
      if (c == 0) {
        ST(1);
        if (w == 0 && lane < 8) stg(tagv, rws, l16, OFF_F1 + (hp * 16 + rb1) * 128);
        const v4u f = trfrag(L + L_WQ + ((lane >> 4) & 1) * 512 + (h * 4 + q4) * 32 + (lane & 3) * 8, 256);
        UNR for (int k = 0; k < 4; ++k) { wp0[k] = (lane & 16) ? 0u : f[k]; wp1[k] = (lane & 16) ? f[k] : 0u; }
      }
      if (c == 7) ST(2);
      UNR for (int ks = 0; ks < 4; ++ks) {
        const v4u x = lds128(xa[ks] + (c & 3) * 16384);
        xf[c * 4 + ks] = x;
        const v4u fk = trfrag(wnat + L_WK + (c * 4 + ks) * 1024, 256), fv = trfrag(wnat + L_WV + (c * 4 + ks) * 1024, 256);
        ak = mfma32(fk, x, ak); av = mfma32(fv, x, av);
        ss0 = dot2(x.x, ONES, ss0); ss1 = dot2(x.y, ONES, ss1); ss0 = dot2(x.z, ONES, ss0); ss1 = dot2(x.w, ONES, ss1);
        sq0 = dot2(x.x, x.x, sq0); sq1 = dot2(x.y, x.y, sq1); sq0 = dot2(x.z, x.z, sq0); sq1 = dot2(x.w, x.w, sq1);
        UNR for (int k = 0; k < 4; ++k) { ck = dot2(fk[k], ONES, ck); cv = dot2(fv[k], ONES, cv); }
      }
      PIN(ss0); PIN(ss1); PIN(sq0); PIN(sq1); PIN(ck); PIN(cv);
    }
    mean = xadd32(ss0 + ss1) * (1.f / 512.f);
    const float var = __builtin_fmaxf(xadd32(sq0 + sq1) * (1.f / 512.f) - mean * mean, 0.f);
    rstd = __builtin_amdgcn_rsqf(var + 1e-5f);
    baug = aug_b(mean, 0.f);
    ak = mfma32(aug_a(ck, 0u), baug, ak);
    av = mfma32(aug_a(cv, 0u), baug, av);
    f32x16 pp = mfma32(wp0, pack8(ak, 0), zero16());
    pp = mfma32(wp1, pack8(ak, 8), pp);
    LAS char* img = L + L_XR + w * 4096 + t * 32 + h * 16;
    av *= rstd;
    *(LAS v4u*)(img + 16384) = pack8(av, 0);
    *(LAS v4u*)(img + 16384 + 1024) = pack8(av, 8);
    UNR for (int e = 0; e < 2; ++e) {
      v4u pos, neg;
      dual_softmax(pp, 8 * e, rstd * 1.44269504f, pos, neg);
      *(LAS v4u*)(img + e * 2048) = pos;
      *(LAS v4u*)(img + e * 2048 + 1024) = neg;
    }
  }
  ST(3);
  bar();
  ST(4);
  {
    f32x4 kv = {0.f, 0.f, 0.f, 0.f}, zz = {0.f, 0.f, 0.f, 0.f};
    const v4u ones = {ONES, ONES, ONES, ONES};
    UNR for (int ww = 0; ww < 4; ++ww) {
      const v4u a = trfrag(L + L_XR + ww * 4096 + w * 1024 + lane * 8, 512);
      const v4u v = trfrag(L + L_XR + 16384 + ww * 4096 + (w >> 1) * 1024 + lane * 8, 512);
      kv = mfma16(a, v, kv); zz = mfma16(a, ones, zz);
    }
    const auto s0 = __builtin_amdgcn_permlane16_swap(pk(kv[0], kv[1]), pk(zz[0], zz[1]), false, false);
    const auto s1 = __builtin_amdgcn_permlane16_swap(pk(kv[2], kv[3]), pk(zz[2], zz[3]), false, false);
    const v4u o = {s0[0], s1[0], s0[1], s1[1]};
    if (t < 20) stg(o, rws, t < 16 ? (h * 16 + t) * 16 : 512 + (h * 4 + t - 16) * 16, kvp_base + rb1 * 640);
  }
  bar();
  ST(5);
  v4u phq[4];
  {
    f32x16 aq = zero16();
    float c0 = 0.f, c1 = 0.f;
    UNR for (int ks = 0; ks < 32; ++ks) {
      const v4u f = trfrag(wnat + L_WQ + ks * 1024, 256);
      aq = mfma32(f, xf[ks], aq);
      c0 = dot2(f.x, ONES, c0); c1 = dot2(f.y, ONES, c1); c0 = dot2(f.z, ONES, c0); c1 = dot2(f.w, ONES, c1);
    }
    aq = mfma32(aug_a(c0 + c1, 0u), baug, aq);
    f32x16 pp = mfma32(wp0, pack8(aq, 0), zero16());
    pp = mfma32(wp1, pack8(aq, 8), pp);
    dual_softmax(pp, 0, rstd * 1.44269504f, phq[0], phq[1]);
    dual_softmax(pp, 8, rstd * 1.44269504f, phq[2], phq[3]);
    PIN(phq[0]); PIN(phq[1]); PIN(phq[2]); PIN(phq[3]);
  }
  ST(6);
  bar();
  ST(7);
  UNR for (int e = 0; e < 2; ++e) {
    f32x16 a = mfma32(lds128(L + L_KVZ + e * 2048 + l16), phq[2 * e], zero16());
    a = mfma32(lds128(L + L_KVZ + e * 2048 + 1024 + l16), phq[2 * e + 1], a);
    const float r = __builtin_amdgcn_rcpf(a[8] + 1e-6f);
    a *= r;
    stg(pack8(a, 0), rws, l16, o_off + e * 1024);
  }
  v4u hres[2];
  {
    f32x16 keep, send = zero16();
    UNR for (int g = 0; g < 4; ++g) {
      const v2u xr = *(LAS v2u*)(L + L_XRES + (tt * 32 + t) * 128 + (((kh * 4 + g) ^ ((t >> 1) & 7)) * 16) + h * 8);
      keep[4 * g] = lo(xr.x); keep[4 * g + 1] = hi(xr.x); keep[4 * g + 2] = lo(xr.y); keep[4 * g + 3] = hi(xr.y);
    }
    LAS char* wkeep = L + L_WO + kh * 32768 + h * 1024 + q4 * 128 + ((kh ^ (q4 >> 1)) * 64) + lsub;
    LAS char* wsend = L + L_WO + kh * 32768 + h * 1024 + q4 * 128 + ((kh ^ 1 ^ (q4 >> 1)) * 64) + lsub;
    const u32 so = OFF_O + ((rb * 4 + th * 2 + tt) * 32 + kh * 16) * 1024;
    v4u f[16];
    ST(8);
    vmwait<0>();
    ST(9);
    load_range<16, 4, 0, 3>(f, rws, l16, so);
    SB();
    sfor<0, 4>([&](auto G) {
      constexpr int g = G.value;
      ensure_group<16, 4, g, 3>(f, rws, l16, so, nretry);
      UNR for (int i = 0; i < 4; ++i) {
        keep = mfma32(trfrag(wkeep + (g * 4 + i) * 2048, 512), f[g * 4 + i], keep);
        send = mfma32(trfrag(wsend + (g * 4 + i) * 2048, 512), f[g * 4 + i], send);
      }
    });
    STN(40, nretry);
    ST(10);
    bar();
    UNR for (int g = 0; g < 4; ++g) *(LAS f32x4*)(L + L_RED + w * 4096 + g * 1024 + l16) = (f32x4){send[4 * g], send[4 * g + 1], send[4 * g + 2], send[4 * g + 3]};
    bar();
    UNR for (int g = 0; g < 4; ++g) {
      const f32x4 r = *(LAS f32x4*)(L + L_RED + (w ^ 2) * 4096 + g * 1024 + l16);
      UNR for (int k = 0; k < 4; ++k) keep[4 * g + k] += r[k];
    }
    hres[0] = pack8(keep, 0); hres[1] = pack8(keep, 8);
    stg(hres[0], rws, l16, h_off); stg(hres[1], rws, l16, h_off + 1024);
    bar();
  }
  v4u pre[6];
  LAS char* const w2keep = L + kh * 2048 + h * 512 + q4 * 128 + ((kh ^ (q4 >> 1)) * 64) + lsub;
  LAS char* const w2send = L + kh * 2048 + h * 512 + q4 * 128 + ((kh ^ 1 ^ (q4 >> 1)) * 64) + lsub;
  {
    const u32 so = OFF_H + (rb * 4 + w) * 32768;
    ST(11);
    vmwait<0>();
    ST(12);
    load_range<32, 8, 0, 3>(xf, rws, l16, so);
    SB();
    bar();
    f32x16 a0 = zero16(), a1 = zero16();
    float ss0 = 0.f, ss1 = 0.f, sq0 = 0.f, sq1 = 0.f;
    sfor<0, 8>([&](auto G) {
      constexpr int g = G.value;
      ensure_group<32, 8, g, g + 3>(xf, rws, l16, so, nretry);
      load_range<32, 8, g + 4, g + 4>(xf, rws, l16, so);
      SB();
      UNR for (int i = 0; i < 4; ++i) {
        const v4u x = xf[g * 4 + i];
        a0 = mfma32(trfrag(wperm + w1_base(0) + (g * 4 + i) * 1024, 512), x, a0);
        a1 = mfma32(trfrag(wperm + w1_base(1) + (g * 4 + i) * 1024, 512), x, a1);
        ss0 = dot2(x.x, ONES, ss0); ss1 = dot2(x.y, ONES, ss1); ss0 = dot2(x.z, ONES, ss0); ss1 = dot2(x.w, ONES, ss1);
        sq0 = dot2(x.x, x.x, sq0); sq1 = dot2(x.y, x.y, sq1); sq0 = dot2(x.z, x.z, sq0); sq1 = dot2(x.w, x.w, sq1);
      }
      PIN(ss0); PIN(ss1); PIN(sq0); PIN(sq1);
    });
    STN(41, nretry);
    const float mu = xadd32(ss0 + ss1) * (1.f / 512.f);
    const float ve = __builtin_fmaxf(xadd32(sq0 + sq1) * (1.f / 512.f) - mu * mu, 0.f) + 1e-5f;
    const float rs = __builtin_amdgcn_rsqf(ve), sd = ve * rs;
    const v4u ba = aug_b(mu, sd);
    const float c1 = -2.302208198f * rs, c2 = c1 * 0.044715f * rs * rs;
    ST(13);
    bar();
    ST(14);
    a0 = mfma32(lds128(L + L_AUG + l16), ba, a0);
    a1 = mfma32(lds128(L + L_AUG + 1024 + l16), ba, a1);
    if (three) {
      f32x16 a2 = mfma32(lds128(L + L_AUG + 2048 + l16), ba, zero16());
      UNR for (int ks = 0; ks < 32; ++ks) a2 = mfma32(trfrag(wperm + w1_base(2) + ks * 1024, 512), xf[ks], a2);
      stg(gelu8(a0, 0, c1, c2, sd), rws, l16, hid_off);
      stg(gelu8(a0, 8, c1, c2, sd), rws, l16, hid_off + 43 * 1024);
      UNR for (int q = 0; q < 3; ++q) { pre[2 * q] = trfrag(w2keep + w2_base(q), 1024); pre[2 * q + 1] = trfrag(w2send + w2_base(q), 1024); }
      ST(15);
      bar();
      stg(gelu8(a1, 0, c1, c2, sd), rws, l16, hid_off + 16384);
      stg(gelu8(a1, 8, c1, c2, sd), rws, l16, hid_off + 16384 + 43 * 1024);
      stg(gelu8(a2, 0, c1, c2, sd), rws, l16, hid_off + 32768);
      stg(gelu8(a2, 8, c1, c2, sd), rws, l16, hid_off + 32768 + 43 * 1024);
    } else {
      stg(gelu8(a0, 0, c1, c2, sd), rws, l16, hid_off);
      stg(gelu8(a0, 8, c1, c2, sd), rws, l16, hid_off + 43 * 1024);
      stg(gelu8(a1, 0, c1, c2, sd), rws, l16, hid_off + 16384);
      stg(gelu8(a1, 8, c1, c2, sd), rws, l16, hid_off + 16384 + 43 * 1024);
      UNR for (int q = 0; q < 3; ++q) { pre[2 * q] = trfrag(w2keep + w2_base(q), 1024); pre[2 * q + 1] = trfrag(w2send + w2_base(q), 1024); }
      ST(15);
      bar();
      vmwait<0>();
    }
  }
  {
    f32x16 keep, send = zero16();
    UNR for (int g = 0; g < 4; ++g) {
      const v2u bb = *(const v2u*)(p.b2 + cg * 64 + kh * 32 + g * 8 + h * 4);
      const u32 r0 = hres[g >> 1][(g & 1) * 2], r1 = hres[g >> 1][(g & 1) * 2 + 1];
      keep[4 * g] = lo(r0) + lo(bb.x); keep[4 * g + 1] = hi(r0) + hi(bb.x); keep[4 * g + 2] = lo(r1) + lo(bb.y); keep[4 * g + 3] = hi(r1) + hi(bb.y);
    }
    const u32 so = OFF_HID + ((rb * 4 + th * 2 + tt) * 86 + kh * 43) * 1024;
    v4u f[24];
    ST(16);
    load_range<43, 6, 0, 4>(f, rws, l16, so);
    SB();
    sfor<0, 11>([&](auto G) {
      constexpr int g = G.value;
      ensure_group<43, 6, g, g + 4>(f, rws, l16, so, nretry);
      load_range<43, 6, g + 5, g + 5>(f, rws, l16, so);
      SB();
      if (g == 0) ST(17);
      UNR for (int i = 0; i < 4; ++i) {
        const int q = g * 4 + i;
        if (q == 15 || q == 31) bar();
        if (q < 43) {
          keep = mfma32(q < 3 ? pre[2 * q] : trfrag(w2keep + w2_base(q), 1024), f[(g % 6) * 4 + i], keep);
          send = mfma32(q < 3 ? pre[2 * q + 1] : trfrag(w2send + w2_base(q), 1024), f[(g % 6) * 4 + i], send);
        }
      }
    });
    STN(42, nretry);
    ST(18);
    bar();
    UNR for (int g = 0; g < 4; ++g) *(LAS f32x4*)(L + 16384 + w * 4096 + g * 1024 + l16) = (f32x4){send[4 * g], send[4 * g + 1], send[4 * g + 2], send[4 * g + 3]};
    bar();
    UNR for (int g = 0; g < 4; ++g) {
      const f32x4 r = *(LAS f32x4*)(L + 16384 + (w ^ 2) * 4096 + g * 1024 + l16);
      UNR for (int k = 0; k < 4; ++k) keep[4 * g + k] += r[k];
    }
    u16* orow = p.out + (size_t)(rb * 128 + th * 64 + tt * 32 + t) * 512 + cg * 64 + kh * 32 + h * 8;
    UNR for (int g = 0; g < 2; ++g) {
      const v4u v = pack8(keep, 8 * g);
      const auto s0 = __builtin_amdgcn_permlane32_swap(v.x, v.z, false, false);
      const auto s1 = __builtin_amdgcn_permlane32_swap(v.y, v.w, false, false);
      __builtin_amdgcn_raw_buffer_store_b128((v4u){s0[0], s1[0], s0[1], s1[1]}, __builtin_amdgcn_make_buffer_rsrc(p.out, 0, 2 << 20, 0x00020000), (u32)((char*)(orow + g * 16) - (char*)p.out), 0, 16);
    }
    if (b == 0 && threadIdx.x == 0) *(u64*)(p.ws + OFF_E) = tag;
  }
  SPIN_TO(60);
  ST_OUT();
}

extern "C" void kernel_launch(void* const* d_in, const int* in_sizes, int n_in, void* d_out, int out_size, void* d_ws, size_t ws_size,
                              hipStream_t stream) {
  Params p;
  p.x = (const u16*)d_in[0]; p.wqkv = (const u16*)d_in[1]; p.wphi = (const u16*)d_in[2]; p.wo = (const u16*)d_in[3];
  p.w1 = (const u16*)d_in[5]; p.b1 = (const u16*)d_in[6]; p.w2 = (const u16*)d_in[7]; p.b2 = (const u16*)d_in[8];
  p.out = (u16*)d_out; p.ws = (char*)d_ws;
  fused_block<<<256, 512, 0, stream>>>(p);
}
```

```cpp
#include <hip/hip_runtime.h>
#include <hip/hip_bf16.h>

typedef unsigned short u16;
typedef unsigned u32;
typedef unsigned long long u64;
typedef u32 v4u __attribute__((ext_vector_type(4)));
typedef u32 v2u __attribute__((ext_vector_type(2)));
typedef float f32x2 __attribute__((ext_vector_type(2)));
typedef float f32x4 __attribute__((ext_vector_type(4)));
typedef float f32x16 __attribute__((ext_vector_type(16)));
typedef __bf16 bf16x2 __attribute__((ext_vector_type(2)));
typedef __bf16 bf16x8 __attribute__((ext_vector_type(8)));
typedef short s16x4 __attribute__((ext_vector_type(4)));
typedef __amdgpu_buffer_rsrc_t rsrc_t;
#define DI __device__ __forceinline__
#define LAS __attribute__((address_space(3)))
#define CAS __attribute__((address_space(4)))
#define UNR _Pragma("unroll")
#define SB() __builtin_amdgcn_sched_barrier(0)

constexpr int OFF_E = 0, OFF_F1 = 4096, OFF_F2A = OFF_F1 + 32768, OFF_F2B = OFF_F2A + 32768, OFF_KVP = OFF_F2B + 32768;
constexpr int OFF_HS = 768 << 10;
constexpr int OFF_O = 1 << 20, OFF_H = 3 << 20, OFF_HID = 5 << 20, WS_BYTES = 11 << 20;
constexpr int OFF_STAMP = 12 << 20;
constexpr int L_WKV = 0, L_WQ = 65536, L_XR = 98304, L_CSP = L_XR + 16384 + 2048;
constexpr int L_WO = 0, L_XRES = 151552, L_KVZ = 159744, L_AUG = 61440, L_STAT = 64512;
constexpr int LDS_BYTES = 163840;
constexpr int w1_off(int j, int ks) { return j == 2 ? 131072 + ks * 1024 : (ks < 16 ? 98304 : 65536) + j * 16384 + (ks & 15) * 1024; }
constexpr int w2_base(int p) { return p < 15 ? p * 4096 : p < 39 ? 65536 + (p - 15) * 4096 : p == 39 ? 61440 : (p - 40) * 4096; }
constexpr int ubase(int m) { return m < 11 ? 3 * m : 2 * m + 11; }
constexpr int SC1 = 16;
constexpr u32 SPIN_CAP = 1u << 16;

DI f32x16 mfma32(v4u a, v4u b, f32x16 c) { return __builtin_amdgcn_mfma_f32_32x32x16_bf16(__builtin_bit_cast(bf16x8, a), __builtin_bit_cast(bf16x8, b), c, 0, 0, 0); }
DI f32x4 mfma16(v4u a, v4u b, f32x4 c) { return __builtin_amdgcn_mfma_f32_16x16x32_bf16(__builtin_bit_cast(bf16x8, a), __builtin_bit_cast(bf16x8, b), c, 0, 0, 0); }
typedef short s16x4v __attribute__((ext_vector_type(4)));
DI f32x4 sum8(v4u f, u32 ones, f32x4 c) {
  const s16x4v o = __builtin_bit_cast(s16x4v, (v2u){ones, ones});
  c = __builtin_amdgcn_mfma_f32_4x4x4bf16_1k(o, __builtin_bit_cast(s16x4v, (v2u){f.x, f.y}), c, 0, 0, 0);
  return __builtin_amdgcn_mfma_f32_4x4x4bf16_1k(o, __builtin_bit_cast(s16x4v, (v2u){f.z, f.w}), c, 0, 0, 0);
}
DI u32 pk(float a, float b) { f32x2 f = {a, b}; return __builtin_bit_cast(u32, __builtin_convertvector(f, bf16x2)); }
DI u32 pk2(f32x2 f) { return __builtin_bit_cast(u32, __builtin_convertvector(f, bf16x2)); }
DI float lo(u32 x) { return __builtin_bit_cast(float, x << 16); }
DI float hi(u32 x) { return __builtin_bit_cast(float, x & 0xffff0000u); }
DI float dot2(u32 a, u32 b, float c) { return __builtin_amdgcn_fdot2_f32_bf16(__builtin_bit_cast(bf16x2, a), __builtin_bit_cast(bf16x2, b), c, false); }
constexpr u32 ONES = 0x3f803f80u;
DI v2u tr8(LAS char* p) { return __builtin_bit_cast(v2u, __builtin_amdgcn_ds_read_tr16_b64_v4i16((LAS s16x4*)p)); }
DI v4u trfrag(LAS char* p, int second) { v2u a = tr8(p), b = tr8(p + second); return (v4u){a.x, a.y, b.x, b.y}; }
DI v4u lds128(LAS char* p) { return *(LAS v4u*)p; }
DI void bar() { SB(); asm volatile("s_waitcnt lgkmcnt(0)\n\ts_barrier" ::: "memory"); SB(); }
DI void bar_nw() { SB(); asm volatile("s_barrier" ::: "memory"); SB(); }
#define PIN(x) asm volatile("" : "+v"(x))
template <int N> DI void vmwait() { asm volatile("s_waitcnt vmcnt(%0)" ::"i"(N) : "memory"); }
DI void glds(u32 lds_addr, u32 voff, rsrc_t rs, u32 soff) {
  asm volatile("s_mov_b32 m0, %0\n\ts_nop 0\n\tbuffer_load_dwordx4 %1, %2, %3 offen lds" ::"s"(lds_addr), "v"(voff), "s"(rs), "s"(soff) : "memory");
}
template <int OFS> DI void glds_o(u32 voff, rsrc_t rs, u32 soff) {
  asm volatile("buffer_load_dwordx4 %0, %1, %2 offen offset:%3 lds" ::"v"(voff), "s"(rs), "s"(soff), "i"(OFS) : "memory");
}
DI LAS char* opq(LAS char* p) { u32 a = (u32)(size_t)p; asm volatile("" : "+v"(a)); return (LAS char*)(size_t)a; }
DI void lda(v4u& d, u32 voff, rsrc_t rs, u32 soff) { asm volatile("buffer_load_dwordx4 %0, %1, %2, %3 offen" : "=v"(d) : "v"(voff), "s"(rs), "s"(soff) : "memory"); }
DI void lda_sc1(v4u& d, u32 voff, rsrc_t rs, u32 soff) { asm volatile("buffer_load_dwordx4 %0, %1, %2, %3 offen sc1" : "=v"(d) : "v"(voff), "s"(rs), "s"(soff) : "memory"); }
DI v4u ldg(rsrc_t rs, u32 voff, u32 soff) { return __builtin_amdgcn_raw_buffer_load_b128(rs, voff, soff, SC1); }
DI void stg(v4u v, rsrc_t rs, u32 voff, u32 soff) { __builtin_amdgcn_raw_buffer_store_b128(v, rs, voff + soff, 0, SC1); }
DI u32 umax3(u32 a, u32 b, u32 c) { return max(max(a, b), c); }
DI u32 mx4(u32 m, v4u d) { return umax3(umax3(m, d.x, d.y), d.z, d.w); }
DI bool anybad(u32 m) { return __builtin_amdgcn_ballot_w64(m == 0xffffffffu) != 0ull; }
DI float fbits(u32 x) { return __builtin_bit_cast(float, x); }
DI u32 ubits(float x) { return __builtin_bit_cast(u32, x); }
DI float fmx(float a, float b) { return __builtin_elementwise_maximum(a, b); }
DI float fmn(float a, float b) { return __builtin_elementwise_minimum(a, b); }
DI float xadd32(float a) { auto r = __builtin_amdgcn_permlane32_swap(ubits(a), ubits(a), false, false); return fbits(r[0]) + fbits(r[1]); }
DI float xmax32(float a) { auto r = __builtin_amdgcn_permlane32_swap(ubits(a), ubits(a), false, false); return fmx(fbits(r[0]), fbits(r[1])); }
DI float xmin32(float a) { auto r = __builtin_amdgcn_permlane32_swap(ubits(a), ubits(a), false, false); return fmn(fbits(r[0]), fbits(r[1])); }
DI float xadd16(float a) { auto r = __builtin_amdgcn_permlane16_swap(ubits(a), ubits(a), false, false); return fbits(r[0]) + fbits(r[1]); }
template <int CTRL> DI float dppadd(float a) { return a + fbits(__builtin_amdgcn_update_dpp(0u, ubits(a), CTRL, 0xf, 0xf, true)); }
DI float rowsum8(float a) { a = dppadd<0x124>(a); a = dppadd<0x122>(a); return dppadd<0x121>(a); }
DI v4u pack8(const f32x16& a, int o) { return (v4u){pk(a[o], a[o + 1]), pk(a[o + 2], a[o + 3]), pk(a[o + 4], a[o + 5]), pk(a[o + 6], a[o + 7])}; }
DI f32x16 zero16() { f32x16 z; UNR for (int i = 0; i < 16; ++i) z[i] = 0.f; return z; }
DI v4u aug_a(float cs, u32 bias_bits) {
  const u32 h2 = pk(cs, cs); const float ch = lo(h2);
  const u32 l2 = pk(cs - ch, 0.f);
  return (v4u){(h2 & 0xffffu) | (l2 << 16), (h2 & 0xffffu) | (bias_bits << 16), bias_bits, 0u};
}
DI v4u aug_b(float mean, float sd) {
  const u32 h2 = pk(-mean, -mean); const u32 s2 = pk(sd, sd);
  const u32 l2 = pk(-mean - lo(h2), sd - lo(s2));
  return (v4u){h2, (l2 & 0xffffu) | (s2 << 16), l2 >> 16, 0u};
}
DI void dual_softmax(const f32x16& p, int o, float c, v4u& pos, v4u& neg) {
  float mx = p[o], mn = p[o];
  UNR for (int j = 1; j < 8; ++j) { mx = fmx(mx, p[o + j]); mn = fmn(mn, p[o + j]); }
  mx = xmax32(mx); mn = xmin32(mn);
  const float cmx = -c * mx, cmn = c * mn;
  f32x2 ep[4], en[4];
  UNR for (int j = 0; j < 4; ++j) {
    const f32x2 v = {p[o + 2 * j], p[o + 2 * j + 1]};
    const f32x2 a = v * c + cmx, b = v * (-c) + cmn;
    ep[j] = (f32x2){__builtin_amdgcn_exp2f(a.x), __builtin_amdgcn_exp2f(a.y)};
    en[j] = (f32x2){__builtin_amdgcn_exp2f(b.x), __builtin_amdgcn_exp2f(b.y)};
  }
  const f32x2 sp2 = (ep[0] + ep[1]) + (ep[2] + ep[3]), sn2 = (en[0] + en[1]) + (en[2] + en[3]);
  const float rp = __builtin_amdgcn_rcpf(xadd32(sp2.x + sp2.y)), rn = __builtin_amdgcn_rcpf(xadd32(sn2.x + sn2.y));
  pos = (v4u){pk2(ep[0] * rp), pk2(ep[1] * rp), pk2(ep[2] * rp), pk2(ep[3] * rp)};
  neg = (v4u){pk2(en[0] * rn), pk2(en[1] * rn), pk2(en[2] * rn), pk2(en[3] * rn)};
}
DI v4u gelu8(const f32x16& a, int o, float c1, float c2, float sd) {
  u32 r[4];
  UNR for (int j = 0; j < 4; ++j) {
    const f32x2 v = {a[o + 2 * j], a[o + 2 * j + 1]};
    const f32x2 g = (v * v * c2 + c1) * v;
    const f32x2 e = {__builtin_amdgcn_exp2f(g.x), __builtin_amdgcn_exp2f(g.y)};
    const f32x2 d = e * sd + sd;
    const f32x2 q = {__builtin_amdgcn_rcpf(d.x), __builtin_amdgcn_rcpf(d.y)};
    r[j] = pk2(v * q);
  }
  return (v4u){r[0], r[1], r[2], r[3]};
}

#define ST(i)
#define STN(i, v)
#define ST_OUT()

#define FS(x)
struct Params {
  const u16 *x, *wqkv, *wphi, *wo, *w1, *b1, *w2, *b2;
  u16* out;
  char* ws;
};

template <int LO, int HI, int R, class LD> DI void load_fr(v4u (&f)[R], LD ld) {
  UNR for (int i = LO; i < HI; ++i) { f[i % R] = ld(i); SB(); }
}
template <int LO, int HI, int R> DI bool bad_fr(v4u (&f)[R]) {
  u32 m = 0;
  UNR for (int i = LO; i < HI; ++i) m = mx4(m, f[i % R]);
  return anybad(m);
}
template <int LO, int HI, int INFL, int R, class LD> DI void ensure(v4u (&f)[R], LD ld, u32& nretry) {
  if (__builtin_expect(bad_fr<LO, HI, R>(f), 0)) {
    u32 spins = 0;
    do {
      asm volatile("" ::: "memory");
      load_fr<LO, HI, R>(f, ld);
      if constexpr (HI < INFL) { if (bad_fr<HI, INFL, R>(f)) load_fr<HI, INFL, R>(f, ld); }
      __builtin_amdgcn_s_waitcnt(0x0F70);
    } while (bad_fr<LO, HI, R>(f) && ++spins < SPIN_CAP);
    nretry += spins + 1;
    SB();
  }
}
template <int I, int N, class F> DI void sfor(F&& f) { if constexpr (I < N) { f(std::integral_constant<int, I>{}); sfor<I + 1, N>(f); } }
constexpr int cmin(int a, int b) { return a < b ? a : b; }
constexpr int CODE_4K = 9;

__global__ void __launch_bounds__(512, 2) fused_block(Params p) {
  __shared__ __attribute__((aligned(1024))) char smem[LDS_BYTES];
  LAS char* L = (LAS char*)smem;
  ST(0);
  const int lane = threadIdx.x & 63, wv = __builtin_amdgcn_readfirstlane(threadIdx.x >> 6), w = wv & 3;
  const int b = blockIdx.x, xc = b & 7, ib = b >> 3;
  u32 cpf = 0;
  if (wv == 4 && ib < CODE_4K) {
    u64 pc0;
    asm volatile("s_getpc_b64 %0" : "=s"(pc0));
    asm volatile("global_load_dword %0, %1, %2" : "+v"(cpf) : "v"(lane * 64), "s"(pc0 + ib * 4096));
  }
  SB();
  const int rb1 = (xc >> 2) * 8 + (ib & 7), hp = (xc & 3) * 4 + (ib >> 3);
  const int rb = xc * 2 + (ib >> 4), m = ib & 15, th = m & 1, cg = m >> 1;
  const bool three = m < 11;
  const int h = lane >> 5, t = lane & 31;
  const u32 l16 = lane * 16;
  const int q4 = (lane & 15) >> 2, lsub = ((lane >> 4) & 1) * 32 + (lane & 3) * 8;
#define WS_SETUP() \
  const rsrc_t rws = __builtin_amdgcn_make_buffer_rsrc(p.ws, 0, WS_BYTES, 0x00020000); \
  const u32 kvp_base = OFF_KVP + (hp * 4 + w) * 16 * 640; \
  const int tt = w & 1, kh = w >> 1; \
  const u32 o_off = OFF_O + ((rb1 * 4 + w) * 32 + hp * 2) * 1024; \
  const u32 h_off = OFF_H + ((rb * 4 + th * 2 + tt) * 32 + cg * 4 + kh * 2) * 1024; \
  const u32 hs_off = OFF_HS + ((rb * 4 + th * 2 + tt) * 16 + cg * 2 + kh) * 256; \
  const u32 hid_off = OFF_HID + ((rb * 4 + w) * 86 + m) * 1024; \
  const u32 hid2 = three ? hid_off + 32768 : WS_BYTES; \
  const u64 tag = *(const CAS u64*)(p.ws + OFF_E) + 0x9E3779B97F4A7C15ull; \
  u32 ones = ONES; asm volatile("" : "+v"(ones));

  if (wv >= 4) {
  __builtin_amdgcn_s_setprio(1);
  WS_SETUP();
  u32 nretry = 0;
  v4u xf[32];
  float rstd, cq;
  v4u baug, wpq;
  {
    LAS char* const wn = opq(L + L_WKV + h * 512 + q4 * 64 + lsub);
    f32x16 ak = zero16(), av = zero16();
    float ss0 = 0.f, ss1 = 0.f, sq0 = 0.f, sq1 = 0.f;
    LAS char* xa[4];
    UNR for (int ks = 0; ks < 4; ++ks) xa[ks] = opq(L + L_XR + w * 4096 + t * 128 + (((2 * ks + h) ^ ((t >> 1) & 7)) * 16));
    LAS char* const csp = opq(L + L_CSP + lane * 4);
    u32 kvo = kvp_base + rb1 * 640 + (t < 16 ? (h * 16 + t) * 16 : t < 20 ? 512 + (h * 4 + t - 16) * 16 : WS_BYTES);
    PIN(kvo);
    auto stats = [&](int c) {
      UNR for (int ks = 0; ks < 4; ++ks) {
        const v4u x = xf[c * 4 + ks];
        ss0 = dot2(x.x, ones, ss0); sq0 = dot2(x.x, x.x, sq0); ss1 = dot2(x.y, ones, ss1); sq1 = dot2(x.y, x.y, sq1);
        ss0 = dot2(x.z, ones, ss0); sq0 = dot2(x.z, x.z, sq0); ss1 = dot2(x.w, ones, ss1); sq1 = dot2(x.w, x.w, sq1);
      }
      PIN(ss0); PIN(ss1); PIN(sq0); PIN(sq1);
    };
    v4u fk[32], fv[32], wpk;
    float ckp[4], cvp[4];
    UNR for (int c = 0; c < 8; ++c) {
      if (c == 3) { vmwait<0>(); asm volatile("" ::"v"(cpf)); }
      FS(ST(22 + c));
      bar();
      FS(ST(30 + c));
      if (c == 0) ST(1);
      if (c == 7) ST(2);
      UNR for (int ks = 0; ks < 4; ++ks) {
        xf[c * 4 + ks] = lds128(xa[ks] + (c & 3) * 16384);
        if (c == 0) { fk[ks] = trfrag(wn + ks * 2048, 256); fv[ks] = trfrag(wn + ks * 2048 + 1024, 256); }
      }
      if (c == 1) {
        wpq = trfrag(L + L_WQ + ((lane >> 4) & 1) * 512 + (h * 4 + q4) * 32 + (lane & 3) * 8, 256);
        wpk = trfrag(L + L_WQ + (lane & 1) * 512 + (h * 4 + q4) * 32 + ((lane >> 4) & 1) * 8 + (lane & 2) * 8, 256);
      }
      if (c == 7) UNR for (int i = 0; i < 4; ++i) { ckp[i] = *(LAS float*)(csp + i * 4096); cvp[i] = *(LAS float*)(csp + i * 4096 + 256); }
      SB();
      if (c > 0) stats(c - 1);
      if (c == 7) stats(7);
      SB();
      if (c < 7) {
        UNR for (int ks = 0; ks < 4; ++ks) {
          const int s = c * 4 + ks;
          ak = mfma32(fk[s], xf[s], ak);
          fk[s + 4] = trfrag(wn + (s + 4) * 2048, 256);
          SB();
          av = mfma32(fv[s], xf[s], av);
          fv[s + 4] = trfrag(wn + (s + 4) * 2048 + 1024, 256);
          SB();
        }
        PIN(ak); PIN(av);
      }
    }
    ak = mfma32(fk[28], xf[28], ak);
    float mean = xadd32(ss0 + ss1) * (1.f / 512.f); PIN(mean);
    SB();
    ak = mfma32(fk[29], xf[29], ak);
    baug = aug_b(mean, 0.f); PIN(baug);
    SB();
    ak = mfma32(fk[30], xf[30], ak);
    float cks = ckp[0] + ckp[1]; PIN(cks); cks += ckp[2]; PIN(cks); cks += ckp[3]; PIN(cks);
    SB();
    ak = mfma32(fk[31], xf[31], ak);
    v4u aga = aug_a(cks, 0u); PIN(aga);
    SB();
    ak = mfma32(aga, baug, ak);
    float msq = xadd32(sq0 + sq1); PIN(msq);
    SB();
    av = mfma32(fv[28], xf[28], av);
    rstd = __builtin_amdgcn_rsqf(__builtin_fmaxf(msq * (1.f / 512.f) - mean * mean, 0.f) + 1e-5f); PIN(rstd);
    SB();
    av = mfma32(fv[29], xf[29], av);
    v4u kb0 = pack8(ak, 0), wz;
    UNR for (int k = 0; k < 4; ++k) wz[k] = (lane & 4) ? 0u : wpk[k];
    SB();
    f32x16 pp = mfma32(wz, kb0, zero16());
    v4u kb1 = pack8(ak, 8);
    UNR for (int k = 0; k < 4; ++k) wz[k] = (lane & 4) ? wpk[k] : 0u;
    SB();
    pp = mfma32(wz, kb1, pp);
    float cvs = cvp[0] + cvp[1]; PIN(cvs); cvs += cvp[2]; PIN(cvs); cvs += cvp[3]; PIN(cvs);
    SB();
    av = mfma32(fv[30], xf[30], av);
    aga = aug_a(cvs, 0u); PIN(aga);
    SB();
    av = mfma32(fv[31], xf[31], av);
    float mx = pp[0], mn = pp[0];
    UNR for (int j = 1; j < 16; ++j) mx = fmx(mx, pp[j]);
    PIN(mx);
    SB();
    av = mfma32(aga, baug, av);
    UNR for (int j = 1; j < 16; ++j) mn = fmn(mn, pp[j]);
    PIN(mn);
    LAS char* const img = opq(L + L_XR + w * 4096 + h * 2048 + t * 32);
    LAS char* const imv = opq(L + L_XR + 16384 + w * 4096 + t * 32 + h * 16);
    SB();
    {
      const float c = rstd * 1.44269504f, cmx = -c * mx, cmn = c * mn;
      UNR for (int sg = 0; sg < 2; ++sg) {
        f32x2 e[8];
        UNR for (int j = 0; j < 8; ++j) {
          const f32x2 v = {pp[2 * j], pp[2 * j + 1]};
          const f32x2 a = sg ? v * (-c) + cmn : v * c + cmx;
          e[j] = (f32x2){__builtin_amdgcn_exp2f(a.x), __builtin_amdgcn_exp2f(a.y)};
        }
        const f32x2 s2 = ((e[0] + e[1]) + (e[2] + e[3])) + ((e[4] + e[5]) + (e[6] + e[7]));
        const float r = __builtin_amdgcn_rcpf(s2.x + s2.y);
        *(LAS v4u*)(img + sg * 1024) = (v4u){pk2(e[0] * r), pk2(e[1] * r), pk2(e[2] * r), pk2(e[3] * r)};
        *(LAS v4u*)(img + sg * 1024 + 16) = (v4u){pk2(e[4] * r), pk2(e[5] * r), pk2(e[6] * r), pk2(e[7] * r)};
        if (sg == 0) {
          av *= rstd;
          *(LAS v4u*)(imv) = pack8(av, 0);
          *(LAS v4u*)(imv + 1024) = pack8(av, 8);
        }
      }
    }
    LAS char* const pa = opq(L + L_XR + w * 1024 + lane * 8);
    LAS char* const pv = opq(L + L_XR + 16384 + (w >> 1) * 1024 + lane * 8);
    v4u one4 = {ones, ones, ones, ones}; PIN(one4);
    ST(3);
    bar();
    ST(4);
    f32x4 kv = {0.f, 0.f, 0.f, 0.f}, zz = {0.f, 0.f, 0.f, 0.f};
    v4u ka[4], kb[4];
    UNR for (int ww = 0; ww < 4; ++ww) { ka[ww] = trfrag(pa + ww * 4096, 512); kb[ww] = trfrag(pv + ww * 4096, 512); SB(); }
    UNR for (int ww = 0; ww < 4; ++ww) { kv = mfma16(ka[ww], kb[ww], kv); zz = mfma16(ka[ww], one4, zz); }
    float cqp[4];
    UNR for (int i = 0; i < 4; ++i) cqp[i] = *(LAS float*)(csp + i * 4096 + 512);
    const auto s0 = __builtin_amdgcn_permlane16_swap(pk(kv[0], kv[1]), pk(zz[0], zz[1]), false, false);
    const auto s1 = __builtin_amdgcn_permlane16_swap(pk(kv[2], kv[3]), pk(zz[2], zz[3]), false, false);
    stg((v4u){s0[0], s1[0], s0[1], s1[1]}, rws, kvo, 0);
    SB();
    cq = (cqp[0] + cqp[1]) + (cqp[2] + cqp[3]);
  }
  bar();
  ST(5);
  v4u phq[4];
  LAS char* const kvz = opq(L + L_KVZ + l16);
  LAS char* xra[4];
  u32 ovo;
  v2u bb[4];
  {
    LAS char* const wq = opq(L + L_WQ + h * 512 + q4 * 64 + lsub);
    v4u f[32], d[9];
    u32 vg = ((lane >> 4) & 1) * 640 + (h * 16 + (lane & 15)) * 16, vz = (((lane >> 4) & 1) * 8 + (lane & 7)) * 640 + 512 + h * 64;
    auto ldk = [&](int i) { return i == 0 ? ldg(rws, vz, kvp_base) : ldg(rws, vg, kvp_base + (i - 1) * 1280); };
    UNR for (int ks = 0; ks < 4; ++ks) f[ks] = trfrag(wq + ks * 1024, 256);
    f32x16 aq = mfma32(aug_a(cq, 0u), baug, zero16());
    SB();
    UNR for (int ks = 0; ks < 32; ++ks) {
      if (ks + 4 < 32) f[ks + 4] = trfrag(wq + (ks + 4) * 1024, 256);
      aq = mfma32(f[ks], xf[ks], aq);
      if (ks == 4) { PIN(vg); PIN(vz); }
      if (ks >= 23) d[ks - 23] = ldk(ks - 23);
      SB();
    }
    bar_nw();
    ST(38);
    UNR for (int g = 0; g < 4; ++g) bb[g] = *(const v2u*)(p.b2 + cg * 64 + kh * 32 + g * 8 + h * 4);
    v4u wp0, wp1;
    UNR for (int k = 0; k < 4; ++k) { wp0[k] = (lane & 16) ? 0u : wpq[k]; wp1[k] = (lane & 16) ? wpq[k] : 0u; }
    f32x16 pp = mfma32(wp0, pack8(aq, 0), zero16());
    pp = mfma32(wp1, pack8(aq, 8), pp);
    UNR for (int g = 0; g < 4; ++g) xra[g] = opq(L + L_XRES + (tt * 32 + t) * 128 + (((kh * 4 + g) ^ ((t >> 1) & 7)) * 16) + h * 8);
    ovo = o_off + l16; PIN(ovo);
    u32 sel0 = 0x00003f80u, sel1 = 0x3f800000u; PIN(sel0); PIN(sel1);
    dual_softmax(pp, 0, rstd * 1.44269504f, phq[0], phq[1]);
    dual_softmax(pp, 8, rstd * 1.44269504f, phq[2], phq[3]);
    PIN(phq[0]); PIN(phq[1]); PIN(phq[2]); PIN(phq[3]);
    ST(39);
    float s[8], zs[8];
    sfor<0, 3>([&](auto G) {
      constexpr int g = G.value, LO = 3 * g, HI = 3 * g + 3;
      ensure<LO, HI, 9, 9>(d, ldk, nretry);
      UNR for (int i = LO; i < HI; ++i) UNR for (int k = 0; k < 4; ++k) {
        if (i == 0) { zs[2 * k] = rowsum8(lo(d[0][k])); zs[2 * k + 1] = rowsum8(hi(d[0][k])); }
        else if (i == 1) { s[2 * k] = lo(d[i][k]); s[2 * k + 1] = hi(d[i][k]); }
        else { s[2 * k] = dot2(d[i][k], sel0, s[2 * k]); s[2 * k + 1] = dot2(d[i][k], sel1, s[2 * k + 1]); }
      }
      UNR for (int k = 0; k < 8; ++k) { PIN(s[k]); if (g == 0) PIN(zs[k]); }
    });
    STN(45, nretry);
    UNR for (int k = 0; k < 8; ++k) { const auto r = __builtin_amdgcn_permlane16_swap(ubits(s[k]), ubits(zs[k]), false, false); s[k] = fbits(r[0]) + fbits(r[1]); }
    *(LAS v4u*)(kvz + w * 1024) = (v4u){pk(s[0], s[1]), pk(s[2], s[3]), pk(s[4], s[5]), pk(s[6], s[7])};
  }
  ST(6);
  bar();
  ST(7);
  f32x16 keep;
  {
    const v4u k0 = lds128(kvz), k1 = lds128(kvz + 1024), k2 = lds128(kvz + 2048), k3 = lds128(kvz + 3072);
    SB();
    v2u xr[4];
    UNR for (int g = 0; g < 4; ++g) xr[g] = *(LAS v2u*)xra[g];
    SB();
    f32x16 a = mfma32(k0, phq[0], zero16());
    a = mfma32(k1, phq[1], a);
    f32x16 c = mfma32(k2, phq[2], zero16());
    c = mfma32(k3, phq[3], c);
    SB();
    float ra = __builtin_amdgcn_rcpf(a[8] + 1e-6f);
    float av[8];
    UNR for (int j = 0; j < 8; ++j) { av[j] = a[j] * ra; PIN(av[j]); }
    stg((v4u){pk(av[0], av[1]), pk(av[2], av[3]), pk(av[4], av[5]), pk(av[6], av[7])}, rws, ovo, 0);
    SB();
    c *= __builtin_amdgcn_rcpf(c[8] + 1e-6f);
    stg(pack8(c, 0), rws, ovo + 1024, 0);
    SB();
    UNR for (int g = 0; g < 4; ++g) { keep[4 * g] = lo(xr[g].x); keep[4 * g + 1] = hi(xr[g].x); keep[4 * g + 2] = lo(xr[g].y); keep[4 * g + 3] = hi(xr[g].y); }
    PIN(keep);
  }
  LAS char* const w01 = opq(L + 65536 + h * 256 + q4 * 64 + lsub);
  {
    f32x16 send = zero16();
    LAS char* const wkeep = opq(L + L_WO + kh * 32768 + h * 1024 + q4 * 128 + ((kh ^ (q4 >> 1)) * 64) + lsub);
    LAS char* const wsend = opq(L + L_WO + kh * 32768 + h * 1024 + q4 * 128 + ((kh ^ 1 ^ (q4 >> 1)) * 64) + lsub);
    LAS char* const redw = opq(L + L_WO + tt * 4096 + kh * 32768 + l16);
    LAS char* const redr = opq(L + L_WO + tt * 4096 + (kh ^ 1) * 32768 + l16);
    u32 hvo = h_off + l16, hso = hs_off + lane * 4; PIN(hvo); PIN(hso);
    v4u wa[16], wb[16];
    UNR for (int s = 0; s < 4; ++s) { wa[s] = trfrag(wkeep + s * 2048, 512); wb[s] = trfrag(wsend + s * 2048, 512); }
    const u32 so = OFF_O + ((rb * 4 + th * 2 + tt) * 32 + kh * 16) * 1024;
    auto ld = [&](int i) { return ldg(rws, l16 + (i & 3) * 1024, so + (i >> 2) * 4096); };
    v4u f[16];
    ST(8);
    vmwait<0>();
    ST(9);
    load_fr<0, 16, 16>(f, ld);
    bar();
    sfor<0, 5>([&](auto G) {
      constexpr int g = G.value, LO = g == 0 ? 0 : 4 * g - 2, HI = cmin(4 * g + 2, 16);
      ensure<LO, HI, 16, 16>(f, ld, nretry);
      UNR for (int s = LO; s < HI; ++s) {
        if (s >= 2 && s + 2 < 16) { wa[s + 2] = trfrag(wkeep + (s + 2) * 2048, 512); wb[s + 2] = trfrag(wsend + (s + 2) * 2048, 512); }
        if (s < 14) { send = mfma32(wb[s], f[s], send); keep = mfma32(wa[s], f[s], keep); }
        SB();
      }
    });
    send = mfma32(wb[14], f[14], send); send = mfma32(wb[15], f[15], send);
    SB();
    keep = mfma32(wa[14], f[14], keep); keep = mfma32(wa[15], f[15], keep);
    SB();
    STN(40, nretry);
    UNR for (int g = 0; g < 4; ++g) *(LAS f32x4*)(redw + g * 1024) = (f32x4){send[4 * g], send[4 * g + 1], send[4 * g + 2], send[4 * g + 3]};
    ST(10);
    bar();
    UNR for (int g = 0; g < 4; ++g) {
      const f32x4 r = *(LAS f32x4*)(redr + g * 1024);
      UNR for (int k = 0; k < 4; ++k) keep[4 * g + k] += r[k];
    }
    const v4u h0 = pack8(keep, 0), h1 = pack8(keep, 8);
    stg(h0, rws, hvo, 0); stg(h1, rws, hvo + 1024, 0);
    SB();
    float ss = 0.f, sq = 0.f;
    UNR for (int k = 0; k < 4; ++k) { ss = dot2(h0[k], ones, ss); sq = dot2(h0[k], h0[k], sq); ss = dot2(h1[k], ones, ss); sq = dot2(h1[k], h1[k], sq); }
    const auto sw = __builtin_amdgcn_permlane32_swap(ubits(ss), ubits(sq), false, false);
    __builtin_amdgcn_raw_buffer_store_b32(ubits(fbits(sw[0]) + fbits(sw[1])), rws, hso, 0, SC1);
    ST(11);
    bar();
    UNR for (int g = 0; g < 4; ++g) {
      const u32 r0 = g < 2 ? h0[g * 2] : h1[g * 2 - 4], r1 = g < 2 ? h0[g * 2 + 1] : h1[g * 2 - 3];
      keep[4 * g] = lo(r0) + lo(bb[g].x); keep[4 * g + 1] = hi(r0) + hi(bb[g].x); keep[4 * g + 2] = lo(r1) + lo(bb[g].y); keep[4 * g + 3] = hi(r1) + hi(bb[g].y);
    }
    PIN(keep);
  }
  v4u wa[43], wb[43], f[20];
  const u32 so4 = OFF_HID + ((rb * 4 + th * 2 + tt) * 86 + kh * 43) * 1024;
  auto ld4 = [&](int i) { return ldg(rws, l16 + (i & 3) * 1024, so4 + (i >> 2) * 4096); };
  const u32 w2k = kh * 2048 + h * 512 + q4 * 128 + ((kh ^ (q4 >> 1)) * 64) + lsub;
  {
    const u32 so = OFF_H + (rb * 4 + w) * 32768;
    auto ld = [&](int i) { return ldg(rws, l16 + (i & 3) * 1024, so + (i >> 2) * 4096); };
    v4u ua[32], ub[32];
    UNR for (int s = 0; s < 2; ++s) { ua[s] = trfrag(w01 + w1_off(0, s) - 65536, 512); ub[s] = trfrag(w01 + w1_off(1, s) - 65536, 512); }
    vmwait<0>();
    ST(12);
    load_fr<0, 16, 32>(xf, ld);
    bar_nw();
    f32x16 a0 = zero16(), a1 = zero16();
    v4u ba, ag0, ag1, ag2; float c1, c2, sd;
    sfor<0, 9>([&](auto G) {
      constexpr int g = G.value, LO = g == 0 ? 0 : 4 * g - 2, HI = cmin(4 * g + 2, 32), INFL = cmin(16 + 4 * g, 32);
      ensure<LO, HI, INFL, 32>(xf, ld, nretry);
      load_fr<INFL, cmin(INFL + 4, 32), 32>(xf, ld);
      SB();
      if (g == 0) ST(13);
      float mu, ms;
      if (g == 5) {
        bar_nw();
        mu = *(LAS float*)(L + L_STAT + w * 256 + t * 4); ms = *(LAS float*)(L + L_STAT + w * 256 + 128 + t * 4);
        ag0 = lds128(L + L_AUG + l16); ag1 = lds128(L + L_AUG + 1024 + l16); ag2 = lds128(L + L_AUG + 2048 + l16);
      }
      UNR for (int s = LO; s < HI; ++s) {
        if (s + 2 < 32) { ua[s + 2] = trfrag(w01 + w1_off(0, s + 2) - 65536, 512); ub[s + 2] = trfrag(w01 + w1_off(1, s + 2) - 65536, 512); }
        if (s < 30) { a0 = mfma32(ua[s], xf[s], a0); a1 = mfma32(ub[s], xf[s], a1); }
        if (g == 5 && s == HI - 2) {
          const float ve = __builtin_fmaxf(ms - mu * mu, 0.f) + 1e-5f;
          const float rs = __builtin_amdgcn_rsqf(ve);
          sd = ve * rs; c1 = -2.302208198f * rs; c2 = c1 * 0.044715f * rs * rs;
          PIN(sd); PIN(c1); PIN(c2);
        }
        if (g == 5 && s == HI - 1) { ba = aug_b(mu, sd); PIN(ba); }
        if (g == 6 && s == LO) { a0 = mfma32(ag0, ba, a0); a1 = mfma32(ag1, ba, a1); }
        SB();
      }
    });
    STN(41, nretry);
    LAS char* const w2u = opq(L + w1_off(2, 0) + h * 256 + q4 * 64 + lsub);
    v4u uc[32];
    a0 = mfma32(ua[30], xf[30], a0); a0 = mfma32(ua[31], xf[31], a0);
    SB();
    a1 = mfma32(ub[30], xf[30], a1);
    UNR for (int s = 0; s < 4; ++s) uc[s] = trfrag(w2u + s * 1024, 512);
    a1 = mfma32(ub[31], xf[31], a1);
    SB();
    ST(14);
    stg(gelu8(a0, 0, c1, c2, sd), rws, l16, hid_off);
    stg(gelu8(a0, 8, c1, c2, sd), rws, l16, hid_off + 43 * 1024);
    bar_nw();
    ST(15);
    LAS char* const wk0 = opq(L + w2k), * const ws0 = opq(L + (w2k ^ 64));
    {
      f32x16 a2 = mfma32(ag2, ba, zero16());
      float g0, e0; v4u r;
      UNR for (int s = 0; s < 32; ++s) {
        if (s + 4 < 32) uc[s + 4] = trfrag(w2u + (s + 4) * 1024, 512);
        else if (s < 31) { wa[s - 28] = trfrag(wk0 + (s - 28) * 4096, 1024); wb[s - 28] = trfrag(ws0 + (s - 28) * 4096, 1024); }
        a2 = mfma32(uc[s], xf[s], a2);
        const float v = a1[s >> 1];
        if (!(s & 1)) { e0 = __builtin_amdgcn_exp2f(__builtin_fmaf(v * v, c2, c1) * v); PIN(e0); }
        else {
          float gg = v * __builtin_amdgcn_rcpf(__builtin_fmaf(e0, sd, sd)); PIN(gg);
          if (s & 2) { r[(s >> 2) & 3] = pk(g0, gg); PIN(r[(s >> 2) & 3]); } else g0 = gg;
          if ((s & 15) == 15) stg(r, rws, l16, hid_off + 16384 + (s >> 4) * 43 * 1024);
        }
        SB();
      }
      ST(16);
      load_fr<0, 8, 20>(f, ld4);
      bar();
      stg(gelu8(a2, 0, c1, c2, sd), rws, l16, hid2);
      load_fr<8, 16, 20>(f, ld4);
      SB();
      stg(gelu8(a2, 8, c1, c2, sd), rws, l16, hid2 + 43 * 1024);
      SB();
    }
  }
  {
    f32x16 send = zero16();
    LAS char* wk[3], * ws[3];
    UNR for (int i = 0; i < 3; ++i) { wk[i] = opq(L + i * 65536 + w2k); ws[i] = opq(L + i * 65536 + (w2k ^ 64)); }
    u16* orow = p.out + (size_t)(rb * 128 + th * 64 + tt * 32 + t) * 512 + cg * 64 + kh * 32 + h * 8;
    PIN(orow);
    LAS char* const redw = opq(L + 16384 + w * 4096 + l16);
    LAS char* const redr = opq(L + 16384 + (w ^ 2) * 4096 + l16);
    ST(17);
    sfor<0, 12>([&](auto G) {
      constexpr int g = G.value, LO = g == 0 ? 0 : g == 11 ? 41 : 4 * g - 2, HI = g == 10 ? 41 : cmin(4 * g + 2, 43), INFL = cmin(LO + 16, 43), NEXT = cmin(HI + 16, 43);
      ensure<LO, HI, INFL, 20>(f, ld4, nretry);
      load_fr<INFL, NEXT, 20>(f, ld4);
      SB();
      if (g == 0) ST(18);
      UNR for (int s = LO; s < HI; ++s) {
        if (s == 13) bar_nw();
        if (s >= 1 && s + 2 < 43) {
          const int o = w2_base(s + 2);
          wa[s + 2] = trfrag(wk[o >> 16] + (o & 65535), 1024); wb[s + 2] = trfrag(ws[o >> 16] + (o & 65535), 1024);
        }
        if (s < 41) { send = mfma32(wb[s], f[s % 20], send); keep = mfma32(wa[s], f[s % 20], keep); }
        SB();
      }
    });
    send = mfma32(wb[41], f[1], send); send = mfma32(wb[42], f[2], send);
    SB();
    keep = mfma32(wa[41], f[1], keep); keep = mfma32(wa[42], f[2], keep);
    SB();
    STN(42, nretry);
    UNR for (int g = 0; g < 4; ++g) *(LAS f32x4*)(redw + g * 1024) = (f32x4){send[4 * g], send[4 * g + 1], send[4 * g + 2], send[4 * g + 3]};
    ST(19);
    bar();
    UNR for (int g = 0; g < 4; ++g) {
      const f32x4 r = *(LAS f32x4*)(redr + g * 1024);
      UNR for (int k = 0; k < 4; ++k) keep[4 * g + k] += r[k];
    }
    UNR for (int g = 0; g < 2; ++g) {
      const v4u v = pack8(keep, 8 * g);
      const auto s0 = __builtin_amdgcn_permlane32_swap(v.x, v.z, false, false);
      const auto s1 = __builtin_amdgcn_permlane32_swap(v.y, v.w, false, false);
      *(__attribute__((address_space(1))) v4u*)(orow + g * 16) = (v4u){s0[0], s1[0], s0[1], s1[1]};
    }
    if (b == 0 && threadIdx.x == 256) *(u64*)(p.ws + OFF_E) = tag;
  }
  ST(20);
  ST_OUT();
  return;
  }

  {
    const u32 lb = (u32)(size_t)L;
    const rsrc_t rx = __builtin_amdgcn_make_buffer_rsrc((void*)p.x, 0, 2048 * 512 * 2, 0x00020000);
    const rsrc_t rq = __builtin_amdgcn_make_buffer_rsrc((void*)p.wqkv, 0, 512 * 1536 * 2, 0x00020000);
    const u32 vx0 = (lane >> 3) * 1024 + (((lane & 7) ^ (lane >> 4)) * 16);
    const u32 vx1 = (vx0 ^ 64) + 7168, vx2 = vx0 + 14336, vx3 = vx1 + 14336;
    const u32 vq = (lane >> 2) * 3072 + (lane & 3) * 16;
    const int rot = rb1 + hp;
    const u32 sq0 = w * 16 * 3072 + hp * 64, sx0 = (rb1 * 128 + w * 32) * 1024;
    auto ldw = [&](int c) {
      const u32 s = sq0 + 1024 + ((c + rot) & 7) * (64 * 3072);
      glds(lb + L_WKV + (c * 4 + w) * 2048, vq, rq, s); glds_o<1024>(vq, rq, s);
    };
    auto ldx = [&](int c) {
      const u32 s = sx0 + ((c + rot) & 7) * 128;
      glds(lb + L_XR + (c & 3) * 16384 + w * 4096, vx0, rx, s); glds_o<1024>(vx1, rx, s); glds_o<2048>(vx2, rx, s); glds_o<3072>(vx3, rx, s);
    };
    auto ldq = [&](int c) { glds(lb + L_WQ + (c * 4 + w) * 1024, vq, rq, sq0 + ((c + rot) & 7) * (64 * 3072)); };
    ldw(0);
    if (w == 0) glds(lb + L_WQ, l16, __builtin_amdgcn_make_buffer_rsrc((void*)p.wphi, 0, 32 * 256 * 2, 0x00020000), hp * 1024);
#pragma unroll 1
    for (int c = 0; c < 3; ++c) { ldw(c + 1); ldx(c); }
    SB();
    ST(13);
    WS_SETUP();
    LAS char* const wn = opq(L + h * 512 + q4 * 64 + lsub + w * 2048);
    f32x4 ck = {0.f, 0.f, 0.f, 0.f}, cv = ck;
    auto csum = [&](int c) { const v4u fk = trfrag(wn + c * 8192, 256), fv = trfrag(wn + c * 8192 + 1024, 256); ck = sum8(fk, ones, ck); cv = sum8(fv, ones, cv); PIN(ck); PIN(cv); };
    v4u sent = {0xffffffffu, 0xffffffffu, 0xffffffffu, 0xffffffffu}; PIN(sent);
    auto fill = [&](int i) {
      if (i == 0) stg(sent, rws, lane < 40 ? l16 + kvp_base + rb1 * 640 : lane < 56 ? l16 - 640 + hs_off : WS_BYTES, 0);
      else stg(sent, rws, l16, i < 3 ? o_off + (i - 1) * 1024 : i < 5 ? h_off + (i - 3) * 1024 : (i < 9 ? hid_off + ((i - 5) >> 1) * 16384 : hid2) + ((i - 5) & 1) * 43 * 1024);
      SB();
    };
    const v4u tagv = {(u32)tag, (u32)(tag >> 32), (u32)tag, (u32)(tag >> 32)};
    __builtin_amdgcn_s_setprio(2);
    UNR for (int c = 0; c < 4; ++c) {
      FS(if (c) ST(14 + c));
      if (c == 0) vmwait<12>(); else if (c == 1) vmwait<13>(); else vmwait<14>();
      FS(ST(22 + c));
      bar(); fill(c); ldw(c + 4); ldx(c + 3); FS(ST(30 + c)); csum(c);
    }
    FS(ST(18)); vmwait<14>(); FS(ST(26)); bar();
    fill(4); ldx(7); ldq(0); ldq(1);
    FS(ST(34));
    csum(4); csum(5);
    FS(ST(19)); vmwait<14>(); FS(ST(27)); bar();
    stg(tagv, rws, w != 0 ? WS_BYTES : (lane & 7) * 16 + (lane < 8 ? OFF_F1 + (hp * 16 + rb1) * 128 : lane < 16 ? OFF_F2A + (rb1 * 16 + hp) * 128 : WS_BYTES), 0);
    fill(5); ldq(2); ldq(3); ldq(4);
    FS(ST(35));
    csum(6);
    FS(ST(20)); vmwait<12>(); FS(ST(28)); bar();
    csum(7);
    *(LAS float*)(L + L_CSP + w * 4096 + lane * 4) = ck[0];
    *(LAS float*)(L + L_CSP + w * 4096 + 256 + lane * 4) = cv[0];
    fill(6); ldq(5); ldq(6); ldq(7);
    FS(ST(21)); vmwait<11>(); FS(ST(29)); bar();
    __builtin_amdgcn_s_setprio(0);
    ST(1);
    const rsrc_t r2 = __builtin_amdgcn_make_buffer_rsrc((void*)p.w2, 0, 1376 * 512 * 2, 0x00020000);
    const u32 vo = (lane >> 3) * 1024 + (((lane & 7) ^ ((lane >> 2) & 4)) * 16);
    const int rt = rb * 2 + th, r12 = rt % 12;
    const u32 s2 = w * 8192 + cg * 128;
    auto w2row = [&](int q) { const int mm = q & 15; return (u32)(min(3 * mm, 2 * mm + 11) + (q >> 4)) * 32768 + s2; };
    auto flags_ok = [&](v4u& fl, u32 fo) {
      u32 spins = 0;
      for (;;) {
        PIN(fl);
        if (__builtin_amdgcn_ballot_w64(fl.x != (u32)tag || fl.y != (u32)(tag >> 32)) == 0ull || ++spins >= SPIN_CAP) break;
        lda_sc1(fl, (lane & 15) * 128, rws, fo);
        vmwait<0>();
      }
      return spins;
    };
    v4u st1[12], fl;
    const u32 fo = (w & 1) ? OFF_F2A + rb * 2048 : OFF_F1 + hp * 2048;
    {
      fill(7); fill(8); fill(9); fill(10);
      vmwait<4>();
      LAS char* const wq = opq(L + L_WQ + h * 512 + q4 * 64 + lsub + w * 1024);
      f32x4 c0 = {0.f, 0.f, 0.f, 0.f}, c1 = c0;
      v4u fq[8];
      UNR for (int c = 0; c < 8; ++c) fq[c] = trfrag(wq + c * 4096, 256);
      SB();
      UNR for (int j = 0; j < 6; ++j) { int q = j + r12; q = q >= 12 ? q - 12 : q; lda(st1[j], vo, r2, w2row(31 + q)); }
      lda_sc1(fl, (lane & 15) * 128, rws, fo);
      UNR for (int c = 0; c < 8; c += 2) { c0 = sum8(fq[c], ones, c0); c1 = sum8(fq[c + 1], ones, c1); }
      *(LAS float*)(L + L_CSP + w * 4096 + 512 + lane * 4) = c0[0] + c1[0];
      UNR for (int j = 6; j < 12; ++j) { int q = j + r12; q = q >= 12 ? q - 12 : q; lda(st1[j], vo, r2, w2row(31 + q)); }
    }
    const rsrc_t ro = __builtin_amdgcn_make_buffer_rsrc((void*)p.wo, 0, 512 * 512 * 2, 0x00020000);
    const rsrc_t r1 = __builtin_amdgcn_make_buffer_rsrc((void*)p.w1, 0, 512 * 1376 * 2, 0x00020000);
    const u32 v1 = (lane >> 2) * 2752 + (lane & 3) * 16;
    const int u0 = min(3 * m, 2 * m + 11);
    const u32 s10 = w * 16 * 2752 + u0 * 64, d2 = three ? 128 : 64;
    auto ldw1 = [&](int n, int j) {
      const int i = (n & 4) | ((n + rb) & 3);
      glds(lb + w1_off(j, (n & 4) * 4) + ((i & 3) * 4 + w) * 1024, v1, r1, s10 + i * (64 * 2752) + (j == 2 ? d2 : j * 64));
    };
    ST(2);
    bar();
#pragma unroll 1
    for (int i = 0; i < 4; ++i) { const int ii = ((i + rt) & 15) * 4 + w; glds(lb + L_WO + ii * 1024, vo, ro, ii * 8192 + cg * 128); }
    vmwait<10>();
    { const u32 sp = flags_ok(fl, fo); STN(40, sp); }
    ST(3);
    bar();
    stg(tagv, rws, (w == 0 && lane < 8) ? l16 + OFF_F2B + (rb * 16 + m) * 128 : WS_BYTES, 0);
#pragma unroll 1
    for (int i = 4; i < 16; ++i) { const int ii = ((i + rt) & 15) * 4 + w; glds(lb + L_WO + ii * 1024, vo, ro, ii * 8192 + cg * 128); }
    { const u32 s = (rb * 128 + th * 64 + w * 16) * 1024 + cg * 128; glds(lb + L_XRES + w * 2048, vx0, rx, s); glds_o<1024>(vx1, rx, s); }
    ST(4);
    bar();
    UNR for (int n = 0; n < 9; ++n) ldw1(n / 3, n % 3);
    vmwait<9>();
    ST(5);
    bar();
    bar();
    u32 bb;
    asm volatile("buffer_load_ushort %0, %1, %2, %3 offen" : "=v"(bb) : "v"(h ? 1376 * 2 : t * 2), "s"(__builtin_amdgcn_make_buffer_rsrc((void*)p.b1, 0, 1376 * 2, 0x00020000)), "s"(min(u0 + w, 42) * 64) : "memory");
    UNR for (int n = 9; n < 24; ++n) ldw1(n / 3, n % 3);
    v4u fl2;
    lda_sc1(fl2, (lane & 15) * 128, rws, OFF_F2B + rb * 2048);
    ST(7);
    bar();
    vmwait<0>();
    { const u32 sp = flags_ok(fl2, OFF_F2B + rb * 2048); STN(44, sp); }
    ST(8);
    bar();
    if (w < 3) {
      LAS char* wp = opq(L + (w == 2 ? 131072 : 98304 + w * 16384) + h * 512 + q4 * 64 + lsub);
      float c0 = 0.f, c1 = 0.f;
#pragma unroll 1
      for (int hf = 0; hf < 4; ++hf) {
        v4u f[8];
        UNR for (int ks = 0; ks < 8; ++ks) f[ks] = trfrag(wp + ks * 1024, 256);
        SB();
        UNR for (int ks = 0; ks < 8; ++ks) { c0 = dot2(f[ks].x, ones, c0); c1 = dot2(f[ks].y, ones, c1); c0 = dot2(f[ks].z, ones, c0); c1 = dot2(f[ks].w, ones, c1); }
        wp += (w != 2 && hf == 1) ? -40960 : 8192;
      }
      PIN(bb);
      *(LAS v4u*)(L + L_AUG + w * 1024 + l16) = aug_a(c0 + c1, bb);
    }
    ST(14);
    bar();
    {
      v4u sv[4]; u32 spins = 0;
      const u32 so = OFF_HS + (rb * 4 + w) * 4096;
      for (;;) {
        UNR for (int i = 0; i < 4; ++i) lda_sc1(sv[i], l16, rws, so + i * 1024);
        vmwait<0>();
        u32 mm = 0;
        UNR for (int i = 0; i < 4; ++i) { PIN(sv[i]); mm = mx4(mm, sv[i]); }
        if (!anybad(mm) || ++spins >= SPIN_CAP) break;
      }
      STN(42, spins);
      f32x4 a;
      UNR for (int k = 0; k < 4; ++k) {
        float x = fbits(sv[0][k]) + fbits(sv[1][k]); PIN(x); x += fbits(sv[2][k]); PIN(x); x += fbits(sv[3][k]); PIN(x);
        x = xadd32(xadd16(x)) * (1.f / 512.f); PIN(x);
        a[k] = x;
      }
      if (lane < 16) *(LAS f32x4*)(L + L_STAT + w * 256 + l16) = a;
    }
    ST(9);
    bar();
    UNR for (int q = 0; q < 3; ++q) glds(lb + q * 4096 + w * 1024, vo, r2, w2row(q));
    {
      int i = r12;
#pragma unroll 1
      for (int n = 0; n < 12; ++n) { glds(lb + (3 + i) * 4096 + w * 1024, vo, r2, w2row(3 + i)); i = i == 11 ? 0 : i + 1; }
    }
    vmwait<12>();
    ST(10);
    bar();
#pragma unroll 1
    for (int n = 0; n < 16; ++n) { const int i = (n + rt) & 15; glds(lb + 65536 + i * 4096 + w * 1024, vo, r2, w2row(15 + i)); }
    vmwait<16>();
    ST(11);
    bar();
    UNR for (int j = 0; j < 12; ++j) {
      int q = j + r12; q = q >= 12 ? q - 12 : q;
      const int base = q < 8 ? 131072 + q * 4096 : q == 8 ? 61440 : (q - 9) * 4096;
      PIN(st1[j]);
      *(LAS v4u*)(L + base + w * 1024 + l16) = st1[j];
    }
    vmwait<0>();
    ST(12);
    bar();
    ST_OUT();
  }
}

extern "C" void kernel_launch(void* const* d_in, const int* in_sizes, int n_in, void* d_out, int out_size, void* d_ws, size_t ws_size,
                              hipStream_t stream) {
  Params p;
  p.x = (const u16*)d_in[0]; p.wqkv = (const u16*)d_in[1]; p.wphi = (const u16*)d_in[2]; p.wo = (const u16*)d_in[3];
  p.w1 = (const u16*)d_in[5]; p.b1 = (const u16*)d_in[6]; p.w2 = (const u16*)d_in[7]; p.b2 = (const u16*)d_in[8];
  p.out = (u16*)d_out; p.ws = (char*)d_ws;
  fused_block<<<256, 512, 0, stream>>>(p);
}
```

```cpp
#include <hip/hip_runtime.h>
#include <hip/hip_bf16.h>

typedef unsigned short u16;
typedef unsigned u32;
typedef unsigned long long u64;
typedef u32 v4u __attribute__((ext_vector_type(4)));
typedef u32 v2u __attribute__((ext_vector_type(2)));
typedef float f32x2 __attribute__((ext_vector_type(2)));
typedef float f32x4 __attribute__((ext_vector_type(4)));
typedef float f32x16 __attribute__((ext_vector_type(16)));
typedef __bf16 bf16x2 __attribute__((ext_vector_type(2)));
typedef __bf16 bf16x8 __attribute__((ext_vector_type(8)));
typedef short s16x4 __attribute__((ext_vector_type(4)));
typedef __amdgpu_buffer_rsrc_t rsrc_t;
#define DI __device__ __forceinline__
#define LAS __attribute__((address_space(3)))
#define CAS __attribute__((address_space(4)))
#define UNR _Pragma("unroll")
#define SB() __builtin_amdgcn_sched_barrier(0)

constexpr int OFF_E = 0, OFF_F1 = 4096, OFF_F2A = OFF_F1 + 32768, OFF_F2B = OFF_F2A + 32768, OFF_KVP = OFF_F2B + 32768;
constexpr int OFF_HS = 768 << 10;
constexpr int OFF_O = 1 << 20, OFF_H = 3 << 20, OFF_HID = 5 << 20, WS_BYTES = 11 << 20;
constexpr int OFF_STAMP = 12 << 20;
constexpr int L_WKV = 0, L_WQ = 65536, L_XR = 98304, L_CSP = L_XR + 16384 + 2048;
constexpr int L_WO = 0, L_XRES = 151552, L_KVZ = 159744, L_AUG = 61440, L_STAT = 64512;
constexpr int LDS_BYTES = 163840;
constexpr int w1_off(int j, int ks) { return j == 2 ? 131072 + ks * 1024 : (ks < 16 ? 98304 : 65536) + j * 16384 + (ks & 15) * 1024; }
constexpr int w2_base(int p) { return p < 15 ? p * 4096 : p < 39 ? 65536 + (p - 15) * 4096 : p == 39 ? 61440 : (p - 40) * 4096; }
constexpr int ubase(int m) { return m < 11 ? 3 * m : 2 * m + 11; }
constexpr int SC1 = 16;
constexpr u32 SPIN_CAP = 1u << 16;

DI f32x16 mfma32(v4u a, v4u b, f32x16 c) { return __builtin_amdgcn_mfma_f32_32x32x16_bf16(__builtin_bit_cast(bf16x8, a), __builtin_bit_cast(bf16x8, b), c, 0, 0, 0); }
DI f32x4 mfma16(v4u a, v4u b, f32x4 c) { return __builtin_amdgcn_mfma_f32_16x16x32_bf16(__builtin_bit_cast(bf16x8, a), __builtin_bit_cast(bf16x8, b), c, 0, 0, 0); }
typedef short s16x4v __attribute__((ext_vector_type(4)));
DI f32x4 sum8(v4u f, u32 ones, f32x4 c) {
  const s16x4v o = __builtin_bit_cast(s16x4v, (v2u){ones, ones});
  c = __builtin_amdgcn_mfma_f32_4x4x4bf16_1k(o, __builtin_bit_cast(s16x4v, (v2u){f.x, f.y}), c, 0, 0, 0);
  return __builtin_amdgcn_mfma_f32_4x4x4bf16_1k(o, __builtin_bit_cast(s16x4v, (v2u){f.z, f.w}), c, 0, 0, 0);
}
DI u32 pk(float a, float b) { f32x2 f = {a, b}; return __builtin_bit_cast(u32, __builtin_convertvector(f, bf16x2)); }
DI u32 pk2(f32x2 f) { return __builtin_bit_cast(u32, __builtin_convertvector(f, bf16x2)); }
DI float lo(u32 x) { return __builtin_bit_cast(float, x << 16); }
DI float hi(u32 x) { return __builtin_bit_cast(float, x & 0xffff0000u); }
DI float dot2(u32 a, u32 b, float c) { return __builtin_amdgcn_fdot2_f32_bf16(__builtin_bit_cast(bf16x2, a), __builtin_bit_cast(bf16x2, b), c, false); }
constexpr u32 ONES = 0x3f803f80u;
DI v2u tr8(LAS char* p) { return __builtin_bit_cast(v2u, __builtin_amdgcn_ds_read_tr16_b64_v4i16((LAS s16x4*)p)); }
DI v4u trfrag(LAS char* p, int second) { v2u a = tr8(p), b = tr8(p + second); return (v4u){a.x, a.y, b.x, b.y}; }
DI v4u lds128(LAS char* p) { return *(LAS v4u*)p; }
DI void bar() { SB(); asm volatile("s_waitcnt lgkmcnt(0)\n\ts_barrier" ::: "memory"); SB(); }
DI void bar_nw() { SB(); asm volatile("s_barrier" ::: "memory"); SB(); }
#define PIN(x) asm volatile("" : "+v"(x))
template <int N> DI void vmwait() { asm volatile("s_waitcnt vmcnt(%0)" ::"i"(N) : "memory"); }
DI void glds(u32 lds_addr, u32 voff, rsrc_t rs, u32 soff) {
  asm volatile("s_mov_b32 m0, %0\n\ts_nop 0\n\tbuffer_load_dwordx4 %1, %2, %3 offen lds" ::"s"(lds_addr), "v"(voff), "s"(rs), "s"(soff) : "memory");
}
template <int OFS> DI void glds_o(u32 voff, rsrc_t rs, u32 soff) {
  asm volatile("buffer_load_dwordx4 %0, %1, %2 offen offset:%3 lds" ::"v"(voff), "s"(rs), "s"(soff), "i"(OFS) : "memory");
}
DI LAS char* opq(LAS char* p) { u32 a = (u32)(size_t)p; asm volatile("" : "+v"(a)); return (LAS char*)(size_t)a; }
DI void lda(v4u& d, u32 voff, rsrc_t rs, u32 soff) { asm volatile("buffer_load_dwordx4 %0, %1, %2, %3 offen" : "=v"(d) : "v"(voff), "s"(rs), "s"(soff) : "memory"); }
DI void lda_sc1(v4u& d, u32 voff, rsrc_t rs, u32 soff) { asm volatile("buffer_load_dwordx4 %0, %1, %2, %3 offen sc1" : "=v"(d) : "v"(voff), "s"(rs), "s"(soff) : "memory"); }
DI v4u ldg(rsrc_t rs, u32 voff, u32 soff) { return __builtin_amdgcn_raw_buffer_load_b128(rs, voff, soff, SC1); }
DI void stg(v4u v, rsrc_t rs, u32 voff, u32 soff) { __builtin_amdgcn_raw_buffer_store_b128(v, rs, voff + soff, 0, SC1); }
DI u32 umax3(u32 a, u32 b, u32 c) { return max(max(a, b), c); }
DI u32 mx4(u32 m, v4u d) { return umax3(umax3(m, d.x, d.y), d.z, d.w); }
DI bool anybad(u32 m) { return __builtin_amdgcn_ballot_w64(m == 0xffffffffu) != 0ull; }
DI float fbits(u32 x) { return __builtin_bit_cast(float, x); }
DI u32 ubits(float x) { return __builtin_bit_cast(u32, x); }
DI float fmx(float a, float b) { return __builtin_elementwise_maximum(a, b); }
DI float fmn(float a, float b) { return __builtin_elementwise_minimum(a, b); }
DI float xadd32(float a) { auto r = __builtin_amdgcn_permlane32_swap(ubits(a), ubits(a), false, false); return fbits(r[0]) + fbits(r[1]); }
DI float xmax32(float a) { auto r = __builtin_amdgcn_permlane32_swap(ubits(a), ubits(a), false, false); return fmx(fbits(r[0]), fbits(r[1])); }
DI float xmin32(float a) { auto r = __builtin_amdgcn_permlane32_swap(ubits(a), ubits(a), false, false); return fmn(fbits(r[0]), fbits(r[1])); }
DI float xadd16(float a) { auto r = __builtin_amdgcn_permlane16_swap(ubits(a), ubits(a), false, false); return fbits(r[0]) + fbits(r[1]); }
template <int CTRL> DI float dppadd(float a) { return a + fbits(__builtin_amdgcn_update_dpp(0u, ubits(a), CTRL, 0xf, 0xf, true)); }
DI float rowsum8(float a) { a = dppadd<0x124>(a); a = dppadd<0x122>(a); return dppadd<0x121>(a); }
DI v4u pack8(const f32x16& a, int o) { return (v4u){pk(a[o], a[o + 1]), pk(a[o + 2], a[o + 3]), pk(a[o + 4], a[o + 5]), pk(a[o + 6], a[o + 7])}; }
DI f32x16 zero16() { f32x16 z; UNR for (int i = 0; i < 16; ++i) z[i] = 0.f; return z; }
DI v4u aug_a(float cs, u32 bias_bits) {
  const u32 h2 = pk(cs, cs); const float ch = lo(h2);
  const u32 l2 = pk(cs - ch, 0.f);
  return (v4u){(h2 & 0xffffu) | (l2 << 16), (h2 & 0xffffu) | (bias_bits << 16), bias_bits, 0u};
}
DI v4u aug_b(float mean, float sd) {
  const u32 h2 = pk(-mean, -mean); const u32 s2 = pk(sd, sd);
  const u32 l2 = pk(-mean - lo(h2), sd - lo(s2));
  return (v4u){h2, (l2 & 0xffffu) | (s2 << 16), l2 >> 16, 0u};
}
DI void dual_softmax(const f32x16& p, int o, float c, v4u& pos, v4u& neg) {
  float mx = p[o], mn = p[o];
  UNR for (int j = 1; j < 8; ++j) { mx = fmx(mx, p[o + j]); mn = fmn(mn, p[o + j]); }
  mx = xmax32(mx); mn = xmin32(mn);
  const float cmx = -c * mx, cmn = c * mn;
  f32x2 ep[4], en[4];
  UNR for (int j = 0; j < 4; ++j) {
    const f32x2 v = {p[o + 2 * j], p[o + 2 * j + 1]};
    const f32x2 a = v * c + cmx, b = v * (-c) + cmn;
    ep[j] = (f32x2){__builtin_amdgcn_exp2f(a.x), __builtin_amdgcn_exp2f(a.y)};
    en[j] = (f32x2){__builtin_amdgcn_exp2f(b.x), __builtin_amdgcn_exp2f(b.y)};
  }
  const f32x2 sp2 = (ep[0] + ep[1]) + (ep[2] + ep[3]), sn2 = (en[0] + en[1]) + (en[2] + en[3]);
  const float rp = __builtin_amdgcn_rcpf(xadd32(sp2.x + sp2.y)), rn = __builtin_amdgcn_rcpf(xadd32(sn2.x + sn2.y));
  pos = (v4u){pk2(ep[0] * rp), pk2(ep[1] * rp), pk2(ep[2] * rp), pk2(ep[3] * rp)};
  neg = (v4u){pk2(en[0] * rn), pk2(en[1] * rn), pk2(en[2] * rn), pk2(en[3] * rn)};
}
DI v4u gelu8(const f32x16& a, int o, float c1, float c2, float sd) {
  u32 r[4];
  UNR for (int j = 0; j < 4; ++j) {
    const f32x2 v = {a[o + 2 * j], a[o + 2 * j + 1]};
    const f32x2 g = (v * v * c2 + c1) * v;
    const f32x2 e = {__builtin_amdgcn_exp2f(g.x), __builtin_amdgcn_exp2f(g.y)};
    const f32x2 d = e * sd + sd;
    const f32x2 q = {__builtin_amdgcn_rcpf(d.x), __builtin_amdgcn_rcpf(d.y)};
    r[j] = pk2(v * q);
  }
  return (v4u){r[0], r[1], r[2], r[3]};
}

#define ST(i)
#define STN(i, v)
#define ST_OUT()

#define FS(x)
struct Params {
  const u16 *x, *wqkv, *wphi, *wo, *w1, *b1, *w2, *b2;
  u16* out;
  char* ws;
};

template <int LO, int HI, int R> DI void load_fr(v4u (&f)[R], rsrc_t rs, u32 voff, u32 soff) {
  UNR for (int i = LO; i < HI; ++i) { f[i % R] = ldg(rs, voff + (i & 3) * 1024, soff + (i >> 2) * 4096); SB(); }
}
template <int LO, int HI, int R> DI bool bad_fr(v4u (&f)[R]) {
  u32 m = 0;
  UNR for (int i = LO; i < HI; ++i) m = mx4(m, f[i % R]);
  return anybad(m);
}
template <int LO, int HI, int INFL, int R> DI void ensure(v4u (&f)[R], rsrc_t rs, u32 voff, u32 soff, u32& nretry) {
  if (__builtin_expect(bad_fr<LO, HI, R>(f), 0)) {
    u32 spins = 0;
    do { asm volatile("" ::: "memory"); load_fr<LO, HI, R>(f, rs, voff, soff); ++spins; } while (bad_fr<LO, HI, R>(f) && spins < SPIN_CAP);
    nretry += spins;
    load_fr<HI, INFL, R>(f, rs, voff, soff);
    SB();
  }
}
template <int I, int N, class F> DI void sfor(F&& f) { if constexpr (I < N) { f(std::integral_constant<int, I>{}); sfor<I + 1, N>(f); } }
constexpr int cmin(int a, int b) { return a < b ? a : b; }
constexpr int CODE_4K = 9;

__global__ void __launch_bounds__(512, 2) fused_block(Params p) {
  __shared__ __attribute__((aligned(1024))) char smem[LDS_BYTES];
  LAS char* L = (LAS char*)smem;
  ST(0);
  const int lane = threadIdx.x & 63, wv = __builtin_amdgcn_readfirstlane(threadIdx.x >> 6), w = wv & 3;
  const int b = blockIdx.x, xc = b & 7, ib = b >> 3;
  u32 cpf = 0;
  if (wv == 4 && ib < CODE_4K) {
    u64 pc0;
    asm volatile("s_getpc_b64 %0" : "=s"(pc0));
    asm volatile("global_load_dword %0, %1, %2" : "+v"(cpf) : "v"(lane * 64), "s"(pc0 + ib * 4096));
  }
  SB();
  const int rb1 = (xc & 1) * 8 + (ib & 7), hp = (xc >> 1) * 4 + (ib >> 3);
  const int rb = xc * 2 + (ib >> 4), m = ib & 15, th = m & 1, cg = m >> 1;
  const bool three = m < 11;
  const int h = lane >> 5, t = lane & 31;
  const u32 l16 = lane * 16;
  const int q4 = (lane & 15) >> 2, lsub = ((lane >> 4) & 1) * 32 + (lane & 3) * 8;
#define WS_SETUP() \
  const rsrc_t rws = __builtin_amdgcn_make_buffer_rsrc(p.ws, 0, WS_BYTES, 0x00020000); \
  const u32 kvp_base = OFF_KVP + (hp * 4 + w) * 16 * 640; \
  const u64 tag = *(const CAS u64*)(p.ws + OFF_E) + 0x9E3779B97F4A7C15ull; \
  u32 ones = ONES; asm volatile("" : "+v"(ones));

  if (wv >= 4) {
  __builtin_amdgcn_s_setprio(1);
  WS_SETUP();
  const int tt = w & 1, kh = w >> 1;
  const u32 o_off = OFF_O + ((rb1 * 4 + w) * 32 + hp * 2) * 1024;
  const u32 h_off = OFF_H + ((rb * 4 + th * 2 + tt) * 32 + cg * 4 + kh * 2) * 1024;
  const u32 hs_off = OFF_HS + ((rb * 4 + th * 2 + tt) * 16 + cg * 2 + kh) * 256;
  const u32 hid_off = OFF_HID + ((rb * 4 + w) * 86 + m) * 1024;
  const u32 hid2 = three ? hid_off + 32768 : WS_BYTES;
  u32 nretry = 0;
  auto fill = [&](int i) {
    const v4u sent = {0xffffffffu, 0xffffffffu, 0xffffffffu, 0xffffffffu};
    if (i == 0) stg(sent, rws, lane < 40 ? l16 + kvp_base + rb1 * 640 : lane < 56 ? l16 - 640 + hs_off : WS_BYTES, 0);
    else stg(sent, rws, l16, i < 3 ? o_off + (i - 1) * 1024 : i < 5 ? h_off + (i - 3) * 1024 : (i < 9 ? hid_off + ((i - 5) >> 1) * 16384 : hid2) + ((i - 5) & 1) * 43 * 1024);
  };
  const v4u tagv = {(u32)tag, (u32)(tag >> 32), (u32)tag, (u32)(tag >> 32)};
  v4u xf[32];
  float rstd, cq;
  v4u baug, wpq;
  {
    LAS char* const wn = opq(L + L_WKV + h * 512 + q4 * 64 + lsub);
    f32x16 ak = zero16(), av = zero16();
    float ss0 = 0.f, ss1 = 0.f, sq0 = 0.f, sq1 = 0.f;
    LAS char* xa[4];
    UNR for (int ks = 0; ks < 4; ++ks) xa[ks] = opq(L + L_XR + w * 4096 + t * 128 + (((2 * ks + h) ^ ((t >> 1) & 7)) * 16));
    LAS char* const csp = opq(L + L_CSP + lane * 4);
    u32 kvo = kvp_base + rb1 * 640 + (t < 16 ? (h * 16 + t) * 16 : t < 20 ? 512 + (h * 4 + t - 16) * 16 : WS_BYTES);
    PIN(kvo);
    auto stats = [&](int c) {
      UNR for (int ks = 0; ks < 4; ++ks) {
        const v4u x = xf[c * 4 + ks];
        ss0 = dot2(x.x, ones, ss0); sq0 = dot2(x.x, x.x, sq0); ss1 = dot2(x.y, ones, ss1); sq1 = dot2(x.y, x.y, sq1);
        ss0 = dot2(x.z, ones, ss0); sq0 = dot2(x.z, x.z, sq0); ss1 = dot2(x.w, ones, ss1); sq1 = dot2(x.w, x.w, sq1);
      }
      PIN(ss0); PIN(ss1); PIN(sq0); PIN(sq1);
    };
    v4u fk[32], fv[32], wpk;
    float ckp[4], cvp[4];
    UNR for (int c = 0; c < 8; ++c) {
      if (c == 5) { vmwait<2>(); asm volatile("" ::"v"(cpf)); }
      FS(ST(22 + c));
      bar();
      FS(ST(30 + c));
      if (c == 0) ST(1);
      if (c == 7) ST(2);
      UNR for (int ks = 0; ks < 4; ++ks) {
        xf[c * 4 + ks] = lds128(xa[ks] + (c & 3) * 16384);
        if (c == 0) { fk[ks] = trfrag(wn + ks * 2048, 256); fv[ks] = trfrag(wn + ks * 2048 + 1024, 256); }
      }
      if (c == 1) {
        wpq = trfrag(L + L_WQ + ((lane >> 4) & 1) * 512 + (h * 4 + q4) * 32 + (lane & 3) * 8, 256);
        wpk = trfrag(L + L_WQ + (lane & 1) * 512 + (h * 4 + q4) * 32 + ((lane >> 4) & 1) * 8 + (lane & 2) * 8, 256);
      }
      if (c == 5 && w == 0) stg(tagv, rws, (lane & 7) * 16 + (lane < 8 ? OFF_F1 + (hp * 16 + rb1) * 128 : lane < 16 ? OFF_F2A + (rb1 * 16 + hp) * 128 : WS_BYTES), 0);
      fill(c);
      if (c == 7) UNR for (int i = 0; i < 4; ++i) { ckp[i] = *(LAS float*)(csp + i * 4096); cvp[i] = *(LAS float*)(csp + i * 4096 + 256); }
      SB();
      if (c > 0) stats(c - 1);
      if (c == 7) stats(7);
      SB();
      if (c < 7) {
        UNR for (int ks = 0; ks < 4; ++ks) {
          const int s = c * 4 + ks;
          ak = mfma32(fk[s], xf[s], ak);
          fk[s + 4] = trfrag(wn + (s + 4) * 2048, 256);
          SB();
          av = mfma32(fv[s], xf[s], av);
          fv[s + 4] = trfrag(wn + (s + 4) * 2048 + 1024, 256);
          SB();
        }
        PIN(ak); PIN(av);
      }
    }
    ak = mfma32(fk[28], xf[28], ak);
    float mean = xadd32(ss0 + ss1) * (1.f / 512.f); PIN(mean);
    SB();
    ak = mfma32(fk[29], xf[29], ak);
    baug = aug_b(mean, 0.f); PIN(baug);
    SB();
    ak = mfma32(fk[30], xf[30], ak);
    float cks = ckp[0] + ckp[1]; PIN(cks); cks += ckp[2]; PIN(cks); cks += ckp[3]; PIN(cks);
    SB();
    ak = mfma32(fk[31], xf[31], ak);
    v4u aga = aug_a(cks, 0u); PIN(aga);
    SB();
    ak = mfma32(aga, baug, ak);
    float msq = xadd32(sq0 + sq1); PIN(msq);
    SB();
    av = mfma32(fv[28], xf[28], av);
    rstd = __builtin_amdgcn_rsqf(__builtin_fmaxf(msq * (1.f / 512.f) - mean * mean, 0.f) + 1e-5f); PIN(rstd);
    SB();
    av = mfma32(fv[29], xf[29], av);
    v4u kb0 = pack8(ak, 0), wz;
    UNR for (int k = 0; k < 4; ++k) wz[k] = (lane & 4) ? 0u : wpk[k];
    SB();
    f32x16 pp = mfma32(wz, kb0, zero16());
    v4u kb1 = pack8(ak, 8);
    UNR for (int k = 0; k < 4; ++k) wz[k] = (lane & 4) ? wpk[k] : 0u;
    SB();
    pp = mfma32(wz, kb1, pp);
    float cvs = cvp[0] + cvp[1]; PIN(cvs); cvs += cvp[2]; PIN(cvs); cvs += cvp[3]; PIN(cvs);
    SB();
    av = mfma32(fv[30], xf[30], av);
    aga = aug_a(cvs, 0u); PIN(aga);
    SB();
    av = mfma32(fv[31], xf[31], av);
    float mx = pp[0], mn = pp[0];
    UNR for (int j = 1; j < 16; ++j) mx = fmx(mx, pp[j]);
    PIN(mx);
    SB();
    av = mfma32(aga, baug, av);
    UNR for (int j = 1; j < 16; ++j) mn = fmn(mn, pp[j]);
    PIN(mn);
    LAS char* const img = opq(L + L_XR + w * 4096 + h * 2048 + t * 32);
    LAS char* const imv = opq(L + L_XR + 16384 + w * 4096 + t * 32 + h * 16);
    SB();
    {
      const float c = rstd * 1.44269504f, cmx = -c * mx, cmn = c * mn;
      UNR for (int sg = 0; sg < 2; ++sg) {
        f32x2 e[8];
        UNR for (int j = 0; j < 8; ++j) {
          const f32x2 v = {pp[2 * j], pp[2 * j + 1]};
          const f32x2 a = sg ? v * (-c) + cmn : v * c + cmx;
          e[j] = (f32x2){__builtin_amdgcn_exp2f(a.x), __builtin_amdgcn_exp2f(a.y)};
        }
        const f32x2 s2 = ((e[0] + e[1]) + (e[2] + e[3])) + ((e[4] + e[5]) + (e[6] + e[7]));
        const float r = __builtin_amdgcn_rcpf(s2.x + s2.y);
        *(LAS v4u*)(img + sg * 1024) = (v4u){pk2(e[0] * r), pk2(e[1] * r), pk2(e[2] * r), pk2(e[3] * r)};
        *(LAS v4u*)(img + sg * 1024 + 16) = (v4u){pk2(e[4] * r), pk2(e[5] * r), pk2(e[6] * r), pk2(e[7] * r)};
        if (sg == 0) {
          av *= rstd;
          *(LAS v4u*)(imv) = pack8(av, 0);
          *(LAS v4u*)(imv + 1024) = pack8(av, 8);
        }
      }
    }
    LAS char* const pa = opq(L + L_XR + w * 1024 + lane * 8);
    LAS char* const pv = opq(L + L_XR + 16384 + (w >> 1) * 1024 + lane * 8);
    v4u one4 = {ones, ones, ones, ones}; PIN(one4);
    ST(3);
    bar();
    ST(4);
    f32x4 kv = {0.f, 0.f, 0.f, 0.f}, zz = {0.f, 0.f, 0.f, 0.f};
    v4u ka[4], kb[4];
    UNR for (int ww = 0; ww < 4; ++ww) { ka[ww] = trfrag(pa + ww * 4096, 512); kb[ww] = trfrag(pv + ww * 4096, 512); SB(); }
    UNR for (int ww = 0; ww < 4; ++ww) { kv = mfma16(ka[ww], kb[ww], kv); zz = mfma16(ka[ww], one4, zz); }
    float cqp[4];
    UNR for (int i = 0; i < 4; ++i) cqp[i] = *(LAS float*)(csp + i * 4096 + 512);
    const auto s0 = __builtin_amdgcn_permlane16_swap(pk(kv[0], kv[1]), pk(zz[0], zz[1]), false, false);
    const auto s1 = __builtin_amdgcn_permlane16_swap(pk(kv[2], kv[3]), pk(zz[2], zz[3]), false, false);
    stg((v4u){s0[0], s1[0], s0[1], s1[1]}, rws, kvo, 0);
    SB();
    cq = (cqp[0] + cqp[1]) + (cqp[2] + cqp[3]);
  }
  bar();
  ST(5);
  v4u phq[4];
  {
    LAS char* const wq = opq(L + L_WQ + h * 512 + q4 * 64 + lsub);
    v4u f[32];
    UNR for (int ks = 0; ks < 4; ++ks) f[ks] = trfrag(wq + ks * 1024, 256);
    f32x16 aq = mfma32(aug_a(cq, 0u), baug, zero16());
    SB();
    UNR for (int ks = 0; ks < 32; ++ks) {
      if (ks + 4 < 32) f[ks + 4] = trfrag(wq + (ks + 4) * 1024, 256);
      aq = mfma32(f[ks], xf[ks], aq);
      if (ks == 24) fill(8);
      SB();
    }
    v4u wp0, wp1;
    UNR for (int k = 0; k < 4; ++k) { wp0[k] = (lane & 16) ? 0u : wpq[k]; wp1[k] = (lane & 16) ? wpq[k] : 0u; }
    f32x16 pp = mfma32(wp0, pack8(aq, 0), zero16());
    pp = mfma32(wp1, pack8(aq, 8), pp);
    fill(9);
    dual_softmax(pp, 0, rstd * 1.44269504f, phq[0], phq[1]);
    SB();
    fill(10);
    dual_softmax(pp, 8, rstd * 1.44269504f, phq[2], phq[3]);
    PIN(phq[0]); PIN(phq[1]); PIN(phq[2]); PIN(phq[3]);
  }
  LAS char* const kvz = opq(L + L_KVZ + l16);
  LAS char* xra[4];
  UNR for (int g = 0; g < 4; ++g) xra[g] = opq(L + L_XRES + (tt * 32 + t) * 128 + (((kh * 4 + g) ^ ((t >> 1) & 7)) * 16) + h * 8);
  u32 ovo = o_off + l16; PIN(ovo);
  v2u bb[4];
  UNR for (int g = 0; g < 4; ++g) bb[g] = *(const v2u*)(p.b2 + cg * 64 + kh * 32 + g * 8 + h * 4);
  ST(6);
  bar();
  ST(7);
  f32x16 keep;
  {
    const v4u k0 = lds128(kvz), k1 = lds128(kvz + 1024), k2 = lds128(kvz + 2048), k3 = lds128(kvz + 3072);
    SB();
    v2u xr[4];
    UNR for (int g = 0; g < 4; ++g) xr[g] = *(LAS v2u*)xra[g];
    SB();
    f32x16 a = mfma32(k0, phq[0], zero16());
    a = mfma32(k1, phq[1], a);
    f32x16 c = mfma32(k2, phq[2], zero16());
    c = mfma32(k3, phq[3], c);
    SB();
    float ra = __builtin_amdgcn_rcpf(a[8] + 1e-6f);
    float av[8];
    UNR for (int j = 0; j < 8; ++j) { av[j] = a[j] * ra; PIN(av[j]); }
    stg((v4u){pk(av[0], av[1]), pk(av[2], av[3]), pk(av[4], av[5]), pk(av[6], av[7])}, rws, ovo, 0);
    SB();
    c *= __builtin_amdgcn_rcpf(c[8] + 1e-6f);
    stg(pack8(c, 0), rws, ovo + 1024, 0);
    SB();
    UNR for (int g = 0; g < 4; ++g) { keep[4 * g] = lo(xr[g].x); keep[4 * g + 1] = hi(xr[g].x); keep[4 * g + 2] = lo(xr[g].y); keep[4 * g + 3] = hi(xr[g].y); }
    PIN(keep);
  }
  LAS char* const w01 = opq(L + 65536 + h * 256 + q4 * 64 + lsub);
  {
    f32x16 send = zero16();
    LAS char* const wkeep = opq(L + L_WO + kh * 32768 + h * 1024 + q4 * 128 + ((kh ^ (q4 >> 1)) * 64) + lsub);
    LAS char* const wsend = opq(L + L_WO + kh * 32768 + h * 1024 + q4 * 128 + ((kh ^ 1 ^ (q4 >> 1)) * 64) + lsub);
    LAS char* const redw = opq(L + L_WO + tt * 4096 + kh * 32768 + l16);
    LAS char* const redr = opq(L + L_WO + tt * 4096 + (kh ^ 1) * 32768 + l16);
    u32 hvo = h_off + l16, hso = hs_off + lane * 4; PIN(hvo); PIN(hso);
    v4u wa[16], wb[16];
    UNR for (int s = 0; s < 4; ++s) { wa[s] = trfrag(wkeep + s * 2048, 512); wb[s] = trfrag(wsend + s * 2048, 512); }
    const u32 so = OFF_O + ((rb * 4 + th * 2 + tt) * 32 + kh * 16) * 1024;
    v4u f[16];
    ST(8);
    vmwait<0>();
    ST(9);
    load_fr<0, 16, 16>(f, rws, l16, so);
    bar();
    stg(tagv, rws, (w == 0 && lane < 8) ? l16 + OFF_F2B + (rb * 16 + m) * 128 : WS_BYTES, 0);
    sfor<0, 5>([&](auto G) {
      constexpr int g = G.value, LO = g == 0 ? 0 : 4 * g - 2, HI = cmin(4 * g + 2, 16);
      ensure<LO, HI, 16, 16>(f, rws, l16, so, nretry);
      UNR for (int s = LO; s < HI; ++s) {
        if (s >= 2 && s + 2 < 16) { wa[s + 2] = trfrag(wkeep + (s + 2) * 2048, 512); wb[s + 2] = trfrag(wsend + (s + 2) * 2048, 512); }
        if (s < 14) { send = mfma32(wb[s], f[s], send); keep = mfma32(wa[s], f[s], keep); }
        SB();
      }
    });
    send = mfma32(wb[14], f[14], send); send = mfma32(wb[15], f[15], send);
    SB();
    keep = mfma32(wa[14], f[14], keep); keep = mfma32(wa[15], f[15], keep);
    SB();
    STN(40, nretry);
    UNR for (int g = 0; g < 4; ++g) *(LAS f32x4*)(redw + g * 1024) = (f32x4){send[4 * g], send[4 * g + 1], send[4 * g + 2], send[4 * g + 3]};
    ST(10);
    bar();
    UNR for (int g = 0; g < 4; ++g) {
      const f32x4 r = *(LAS f32x4*)(redr + g * 1024);
      UNR for (int k = 0; k < 4; ++k) keep[4 * g + k] += r[k];
    }
    const v4u h0 = pack8(keep, 0), h1 = pack8(keep, 8);
    stg(h0, rws, hvo, 0); stg(h1, rws, hvo + 1024, 0);
    SB();
    float ss = 0.f, sq = 0.f;
    UNR for (int k = 0; k < 4; ++k) { ss = dot2(h0[k], ones, ss); sq = dot2(h0[k], h0[k], sq); ss = dot2(h1[k], ones, ss); sq = dot2(h1[k], h1[k], sq); }
    const auto sw = __builtin_amdgcn_permlane32_swap(ubits(ss), ubits(sq), false, false);
    __builtin_amdgcn_raw_buffer_store_b32(ubits(fbits(sw[0]) + fbits(sw[1])), rws, hso, 0, SC1);
    ST(11);
    bar();
    UNR for (int g = 0; g < 4; ++g) {
      const u32 r0 = g < 2 ? h0[g * 2] : h1[g * 2 - 4], r1 = g < 2 ? h0[g * 2 + 1] : h1[g * 2 - 3];
      keep[4 * g] = lo(r0) + lo(bb[g].x); keep[4 * g + 1] = hi(r0) + hi(bb[g].x); keep[4 * g + 2] = lo(r1) + lo(bb[g].y); keep[4 * g + 3] = hi(r1) + hi(bb[g].y);
    }
    PIN(keep);
  }
  v4u wa[43], wb[43], f[20];
  const u32 so4 = OFF_HID + ((rb * 4 + th * 2 + tt) * 86 + kh * 43) * 1024;
  const u32 w2k = kh * 2048 + h * 512 + q4 * 128 + ((kh ^ (q4 >> 1)) * 64) + lsub;
  {
    const u32 so = OFF_H + (rb * 4 + w) * 32768;
    v4u ua[32], ub[32];
    UNR for (int s = 0; s < 2; ++s) { ua[s] = trfrag(w01 + w1_off(0, s) - 65536, 512); ub[s] = trfrag(w01 + w1_off(1, s) - 65536, 512); }
    vmwait<0>();
    ST(12);
    load_fr<0, 16, 32>(xf, rws, l16, so);
    bar_nw();
    f32x16 a0 = zero16(), a1 = zero16();
    v4u ba, ag0, ag1, ag2; float c1, c2, sd;
    sfor<0, 9>([&](auto G) {
      constexpr int g = G.value, LO = g == 0 ? 0 : 4 * g - 2, HI = cmin(4 * g + 2, 32), INFL = cmin(16 + 4 * g, 32);
      ensure<LO, HI, INFL, 32>(xf, rws, l16, so, nretry);
      load_fr<INFL, cmin(INFL + 4, 32), 32>(xf, rws, l16, so);
      SB();
      if (g == 0) ST(13);
      float mu, ms;
      if (g == 5) {
        bar_nw();
        mu = *(LAS float*)(L + L_STAT + w * 256 + t * 4); ms = *(LAS float*)(L + L_STAT + w * 256 + 128 + t * 4);
        ag0 = lds128(L + L_AUG + l16); ag1 = lds128(L + L_AUG + 1024 + l16); ag2 = lds128(L + L_AUG + 2048 + l16);
      }
      UNR for (int s = LO; s < HI; ++s) {
        if (s + 2 < 32) { ua[s + 2] = trfrag(w01 + w1_off(0, s + 2) - 65536, 512); ub[s + 2] = trfrag(w01 + w1_off(1, s + 2) - 65536, 512); }
        if (s < 30) { a0 = mfma32(ua[s], xf[s], a0); a1 = mfma32(ub[s], xf[s], a1); }
        if (g == 5 && s == HI - 2) {
          const float ve = __builtin_fmaxf(ms - mu * mu, 0.f) + 1e-5f;
          const float rs = __builtin_amdgcn_rsqf(ve);
          sd = ve * rs; c1 = -2.302208198f * rs; c2 = c1 * 0.044715f * rs * rs;
          PIN(sd); PIN(c1); PIN(c2);
        }
        if (g == 5 && s == HI - 1) { ba = aug_b(mu, sd); PIN(ba); }
        if (g == 6 && s == LO) { a0 = mfma32(ag0, ba, a0); a1 = mfma32(ag1, ba, a1); }
        SB();
      }
    });
    STN(41, nretry);
    LAS char* const w2u = opq(L + w1_off(2, 0) + h * 256 + q4 * 64 + lsub);
    v4u uc[32];
    a0 = mfma32(ua[30], xf[30], a0); a0 = mfma32(ua[31], xf[31], a0);
    SB();
    a1 = mfma32(ub[30], xf[30], a1);
    UNR for (int s = 0; s < 4; ++s) uc[s] = trfrag(w2u + s * 1024, 512);
    a1 = mfma32(ub[31], xf[31], a1);
    SB();
    ST(14);
    stg(gelu8(a0, 0, c1, c2, sd), rws, l16, hid_off);
    stg(gelu8(a0, 8, c1, c2, sd), rws, l16, hid_off + 43 * 1024);
    bar_nw();
    ST(15);
    LAS char* const wk0 = opq(L + w2k), * const ws0 = opq(L + (w2k ^ 64));
    {
      f32x16 a2 = mfma32(ag2, ba, zero16());
      float g0, e0; v4u r;
      UNR for (int s = 0; s < 32; ++s) {
        if (s + 4 < 32) uc[s + 4] = trfrag(w2u + (s + 4) * 1024, 512);
        else if (s < 31) { wa[s - 28] = trfrag(wk0 + (s - 28) * 4096, 1024); wb[s - 28] = trfrag(ws0 + (s - 28) * 4096, 1024); }
        a2 = mfma32(uc[s], xf[s], a2);
        const float v = a1[s >> 1];
        if (!(s & 1)) { e0 = __builtin_amdgcn_exp2f(__builtin_fmaf(v * v, c2, c1) * v); PIN(e0); }
        else {
          float gg = v * __builtin_amdgcn_rcpf(__builtin_fmaf(e0, sd, sd)); PIN(gg);
          if (s & 2) { r[(s >> 2) & 3] = pk(g0, gg); PIN(r[(s >> 2) & 3]); } else g0 = gg;
          if ((s & 15) == 15) stg(r, rws, l16, hid_off + 16384 + (s >> 4) * 43 * 1024);
        }
        SB();
      }
      ST(16);
      load_fr<0, 8, 20>(f, rws, l16, so4);
      bar();
      stg(gelu8(a2, 0, c1, c2, sd), rws, l16, hid2);
      load_fr<8, 16, 20>(f, rws, l16, so4);
      SB();
      stg(gelu8(a2, 8, c1, c2, sd), rws, l16, hid2 + 43 * 1024);
      SB();
    }
  }
  {
    f32x16 send = zero16();
    LAS char* wk[3], * ws[3];
    UNR for (int i = 0; i < 3; ++i) { wk[i] = opq(L + i * 65536 + w2k); ws[i] = opq(L + i * 65536 + (w2k ^ 64)); }
    u16* orow = p.out + (size_t)(rb * 128 + th * 64 + tt * 32 + t) * 512 + cg * 64 + kh * 32 + h * 8;
    PIN(orow);
    LAS char* const redw = opq(L + 16384 + w * 4096 + l16);
    LAS char* const redr = opq(L + 16384 + (w ^ 2) * 4096 + l16);
    ST(17);
    sfor<0, 12>([&](auto G) {
      constexpr int g = G.value, LO = g == 0 ? 0 : g == 11 ? 41 : 4 * g - 2, HI = g == 10 ? 41 : cmin(4 * g + 2, 43), INFL = cmin(LO + 16, 43), NEXT = cmin(HI + 16, 43);
      ensure<LO, HI, INFL, 20>(f, rws, l16, so4, nretry);
      load_fr<INFL, NEXT, 20>(f, rws, l16, so4);
      SB();
      if (g == 0) ST(18);
      UNR for (int s = LO; s < HI; ++s) {
        if (s == 13) bar_nw();
        if (s >= 1 && s + 2 < 43) {
          const int o = w2_base(s + 2);
          wa[s + 2] = trfrag(wk[o >> 16] + (o & 65535), 1024); wb[s + 2] = trfrag(ws[o >> 16] + (o & 65535), 1024);
        }
        if (s < 41) { send = mfma32(wb[s], f[s % 20], send); keep = mfma32(wa[s], f[s % 20], keep); }
        SB();
      }
    });
    send = mfma32(wb[41], f[1], send); send = mfma32(wb[42], f[2], send);
    SB();
    keep = mfma32(wa[41], f[1], keep); keep = mfma32(wa[42], f[2], keep);
    SB();
    STN(42, nretry);
    UNR for (int g = 0; g < 4; ++g) *(LAS f32x4*)(redw + g * 1024) = (f32x4){send[4 * g], send[4 * g + 1], send[4 * g + 2], send[4 * g + 3]};
    ST(19);
    bar();
    UNR for (int g = 0; g < 4; ++g) {
      const f32x4 r = *(LAS f32x4*)(redr + g * 1024);
      UNR for (int k = 0; k < 4; ++k) keep[4 * g + k] += r[k];
    }
    UNR for (int g = 0; g < 2; ++g) {
      const v4u v = pack8(keep, 8 * g);
      const auto s0 = __builtin_amdgcn_permlane32_swap(v.x, v.z, false, false);
      const auto s1 = __builtin_amdgcn_permlane32_swap(v.y, v.w, false, false);
      *(__attribute__((address_space(1))) v4u*)(orow + g * 16) = (v4u){s0[0], s1[0], s0[1], s1[1]};
    }
    if (b == 0 && threadIdx.x == 256) *(u64*)(p.ws + OFF_E) = tag;
  }
  ST(20);
  ST_OUT();
  return;
  }

  {
    const u32 lb = (u32)(size_t)L;
    const rsrc_t rx = __builtin_amdgcn_make_buffer_rsrc((void*)p.x, 0, 2048 * 512 * 2, 0x00020000);
    const rsrc_t rq = __builtin_amdgcn_make_buffer_rsrc((void*)p.wqkv, 0, 512 * 1536 * 2, 0x00020000);
    const u32 vx0 = (lane >> 3) * 1024 + (((lane & 7) ^ (lane >> 4)) * 16);
    const u32 vx1 = (vx0 ^ 64) + 7168, vx2 = vx0 + 14336, vx3 = vx1 + 14336;
    const u32 vq = (lane >> 2) * 3072 + (lane & 3) * 16;
    const int rot = rb1 + hp;
    const u32 sq0 = w * 16 * 3072 + hp * 64, sx0 = (rb1 * 128 + w * 32) * 1024;
    auto ldw = [&](int c) {
      const u32 s = sq0 + 1024 + ((c + rot) & 7) * (64 * 3072);
      glds(lb + L_WKV + (c * 4 + w) * 2048, vq, rq, s); glds_o<1024>(vq, rq, s);
    };
    auto ldx = [&](int c) {
      const u32 s = sx0 + ((c + rot) & 7) * 128;
      glds(lb + L_XR + (c & 3) * 16384 + w * 4096, vx0, rx, s); glds_o<1024>(vx1, rx, s); glds_o<2048>(vx2, rx, s); glds_o<3072>(vx3, rx, s);
    };
    auto ldq = [&](int c) { glds(lb + L_WQ + (c * 4 + w) * 1024, vq, rq, sq0 + ((c + rot) & 7) * (64 * 3072)); };
    ldw(0);
    if (w == 0) glds(lb + L_WQ, l16, __builtin_amdgcn_make_buffer_rsrc((void*)p.wphi, 0, 32 * 256 * 2, 0x00020000), hp * 1024);
#pragma unroll 1
    for (int c = 0; c < 3; ++c) { ldw(c + 1); ldx(c); }
    SB();
    ST(13);
    WS_SETUP();
    LAS char* const wn = opq(L + h * 512 + q4 * 64 + lsub + w * 2048);
    f32x4 ck = {0.f, 0.f, 0.f, 0.f}, cv = ck;
    auto csum = [&](int c) { const v4u fk = trfrag(wn + c * 8192, 256), fv = trfrag(wn + c * 8192 + 1024, 256); ck = sum8(fk, ones, ck); cv = sum8(fv, ones, cv); PIN(ck); PIN(cv); };
    __builtin_amdgcn_s_setprio(2);
#pragma unroll 1
    for (int c = 0; c < 4; ++c) { vmwait<12>(); bar(); ldw(c + 4); ldx(c + 3); csum(c); }
    FS(ST(18)); vmwait<12>(); FS(ST(26)); bar();
    ldx(7); ldq(0); ldq(1);
    FS(ST(34));
    csum(4); csum(5);
    FS(ST(19)); vmwait<12>(); FS(ST(27)); bar();
    ldq(2); ldq(3); ldq(4);
    FS(ST(35));
    csum(6);
    FS(ST(20)); vmwait<9>(); FS(ST(28)); bar();
    csum(7);
    *(LAS float*)(L + L_CSP + w * 4096 + lane * 4) = ck[0];
    *(LAS float*)(L + L_CSP + w * 4096 + 256 + lane * 4) = cv[0];
    ldq(5); ldq(6); ldq(7);
    FS(ST(21)); vmwait<8>(); FS(ST(29)); bar();
    ST(1);
    const rsrc_t r2 = __builtin_amdgcn_make_buffer_rsrc((void*)p.w2, 0, 1376 * 512 * 2, 0x00020000);
    const u32 vo = (lane >> 3) * 1024 + (((lane & 7) ^ ((lane >> 2) & 4)) * 16);
    const int rt = rb * 2 + th;
    const u32 s2 = w * 8192 + cg * 128;
    auto w2row = [&](int q) { const int mm = q & 15; return (u32)(min(3 * mm, 2 * mm + 11) + (q >> 4)) * 32768 + s2; };
    auto flags_ok = [&](v4u& fl, u32 fo) {
      u32 spins = 0;
      for (;;) {
        PIN(fl);
        if (__builtin_amdgcn_ballot_w64(fl.x != (u32)tag || fl.y != (u32)(tag >> 32)) == 0ull || ++spins >= SPIN_CAP) break;
        lda_sc1(fl, (lane & 15) * 128, rws, fo);
        vmwait<0>();
      }
      return spins;
    };
    v4u st1[16];
    {
      const u32 fo = (w & 1) ? OFF_F2A + rb * 2048 : OFF_F1 + hp * 2048;
      v4u fl;
      UNR for (int j = 0; j < 4; ++j) lda(st1[j], vo, r2, w2row(27 + ((j + rt) & 15)));
      lda_sc1(fl, (lane & 15) * 128, rws, fo);
      UNR for (int j = 4; j < 16; ++j) lda(st1[j], vo, r2, w2row(27 + ((j + rt) & 15)));
      vmwait<12>();
      LAS char* const wq = opq(L + L_WQ + h * 512 + q4 * 64 + lsub + w * 1024);
      f32x4 c0 = {0.f, 0.f, 0.f, 0.f}, c1 = c0;
      v4u fq[8];
      UNR for (int c = 0; c < 8; ++c) fq[c] = trfrag(wq + c * 4096, 256);
      SB();
      UNR for (int c = 0; c < 8; c += 2) { c0 = sum8(fq[c], ones, c0); c1 = sum8(fq[c + 1], ones, c1); }
      *(LAS float*)(L + L_CSP + w * 4096 + 512 + lane * 4) = c0[0] + c1[0];
      const u32 sp = flags_ok(fl, fo);
      STN(40, sp);
    }
    const rsrc_t ro = __builtin_amdgcn_make_buffer_rsrc((void*)p.wo, 0, 512 * 512 * 2, 0x00020000);
    const rsrc_t r1 = __builtin_amdgcn_make_buffer_rsrc((void*)p.w1, 0, 512 * 1376 * 2, 0x00020000);
    const u32 v1 = (lane >> 2) * 2752 + (lane & 3) * 16;
    const int u0 = min(3 * m, 2 * m + 11);
    const u32 s10 = w * 16 * 2752 + u0 * 64, d2 = three ? 128 : 64;
    auto ldw1 = [&](int n, int j) {
      const int i = (n & 4) | ((n + rb) & 3);
      glds(lb + w1_off(j, (n & 4) * 4) + ((i & 3) * 4 + w) * 1024, v1, r1, s10 + i * (64 * 2752) + (j == 2 ? d2 : j * 64));
    };
    ST(2);
    bar();
#pragma unroll 1
    for (int i = 0; i < 5; ++i) { const int ii = ((i + rt) & 15) * 4 + w; glds(lb + L_WO + ii * 1024, vo, ro, ii * 8192 + cg * 128); }
    bar();
#pragma unroll 1
    for (int i = 5; i < 16; ++i) { const int ii = ((i + rt) & 15) * 4 + w; glds(lb + L_WO + ii * 1024, vo, ro, ii * 8192 + cg * 128); }
    { const u32 s = (rb * 128 + th * 64 + w * 16) * 1024 + cg * 128; glds(lb + L_XRES + w * 2048, vx0, rx, s); glds_o<1024>(vx1, rx, s); }
    ST(3);
    {
      v4u d[8], z; u32 spins = 0;
      const u32 vg = ((lane >> 4) & 1) * 640 + (h * 16 + (lane & 15)) * 16, vz = (((lane >> 4) & 1) * 8 + (lane & 7)) * 640 + 512 + h * 64;
      u32 sel0 = 0x00003f80u, sel1 = 0x3f800000u; PIN(sel0); PIN(sel1);
      lda_sc1(z, vz, rws, kvp_base);
      UNR for (int i = 0; i < 8; ++i) lda_sc1(d[i], vg, rws, kvp_base + i * 1280);
      UNR for (int n = 0; n < 4; ++n) ldw1(n / 3, n % 3);
      float s[8], zs[8];
      for (;;) {
        vmwait<12>(); PIN(z);
        u32 mm = mx4(0, z);
        UNR for (int k = 0; k < 4; ++k) { zs[2 * k] = rowsum8(lo(z[k])); zs[2 * k + 1] = rowsum8(hi(z[k])); }
        UNR for (int k = 0; k < 8; ++k) PIN(zs[k]);
        UNR for (int i = 0; i < 8; ++i) {
          vmwait<11>();
          PIN(d[i]);
          if (i < 7 && spins == 0) ldw1((4 + i) / 3, (4 + i) % 3);
          mm = mx4(mm, d[i]);
          UNR for (int k = 0; k < 4; ++k) {
            if (i == 0) { s[2 * k] = lo(d[i][k]); s[2 * k + 1] = hi(d[i][k]); }
            else { s[2 * k] = dot2(d[i][k], sel0, s[2 * k]); s[2 * k + 1] = dot2(d[i][k], sel1, s[2 * k + 1]); }
          }
          UNR for (int k = 0; k < 8; ++k) PIN(s[k]);
          PIN(mm);
        }
        if (!anybad(mm) || ++spins >= SPIN_CAP) break;
        lda_sc1(z, vz, rws, kvp_base);
        UNR for (int i = 0; i < 8; ++i) lda_sc1(d[i], vg, rws, kvp_base + i * 1280);
        vmwait<0>();
      }
      STN(41, spins);
      ST(4);
      UNR for (int k = 0; k < 8; ++k) { const auto r = __builtin_amdgcn_permlane16_swap(ubits(s[k]), ubits(zs[k]), false, false); s[k] = fbits(r[0]) + fbits(r[1]); }
      *(LAS v4u*)(L + L_KVZ + w * 1024 + l16) = (v4u){pk(s[0], s[1]), pk(s[2], s[3]), pk(s[4], s[5]), pk(s[6], s[7])};
    }
    ST(5);
    bar();
    __builtin_amdgcn_s_setprio(0);
    bar();
    u32 bb;
    asm volatile("buffer_load_ushort %0, %1, %2, %3 offen" : "=v"(bb) : "v"(h ? 1376 * 2 : t * 2), "s"(__builtin_amdgcn_make_buffer_rsrc((void*)p.b1, 0, 1376 * 2, 0x00020000)), "s"(min(u0 + w, 42) * 64) : "memory");
    UNR for (int n = 11; n < 24; ++n) ldw1(n / 3, n % 3);
    v4u fl2;
    lda_sc1(fl2, (lane & 15) * 128, rws, OFF_F2B + rb * 2048);
    ST(7);
    bar();
    vmwait<0>();
    { const u32 sp = flags_ok(fl2, OFF_F2B + rb * 2048); STN(44, sp); }
    ST(8);
    bar();
    if (w < 3) {
      LAS char* wp = opq(L + (w == 2 ? 131072 : 98304 + w * 16384) + h * 512 + q4 * 64 + lsub);
      float c0 = 0.f, c1 = 0.f;
#pragma unroll 1
      for (int hf = 0; hf < 4; ++hf) {
        v4u f[8];
        UNR for (int ks = 0; ks < 8; ++ks) f[ks] = trfrag(wp + ks * 1024, 256);
        SB();
        UNR for (int ks = 0; ks < 8; ++ks) { c0 = dot2(f[ks].x, ones, c0); c1 = dot2(f[ks].y, ones, c1); c0 = dot2(f[ks].z, ones, c0); c1 = dot2(f[ks].w, ones, c1); }
        wp += (w != 2 && hf == 1) ? -40960 : 8192;
      }
      PIN(bb);
      *(LAS v4u*)(L + L_AUG + w * 1024 + l16) = aug_a(c0 + c1, bb);
    }
    ST(14);
    bar();
    {
      v4u sv[4]; u32 spins = 0;
      const u32 so = OFF_HS + (rb * 4 + w) * 4096;
      for (;;) {
        UNR for (int i = 0; i < 4; ++i) lda_sc1(sv[i], l16, rws, so + i * 1024);
        vmwait<0>();
        u32 mm = 0;
        UNR for (int i = 0; i < 4; ++i) { PIN(sv[i]); mm = mx4(mm, sv[i]); }
        if (!anybad(mm) || ++spins >= SPIN_CAP) break;
      }
      STN(42, spins);
      f32x4 a;
      UNR for (int k = 0; k < 4; ++k) {
        float x = fbits(sv[0][k]) + fbits(sv[1][k]); PIN(x); x += fbits(sv[2][k]); PIN(x); x += fbits(sv[3][k]); PIN(x);
        x = xadd32(xadd16(x)) * (1.f / 512.f); PIN(x);
        a[k] = x;
      }
      if (lane < 16) *(LAS f32x4*)(L + L_STAT + w * 256 + l16) = a;
    }
    ST(9);
    bar();
    const int r12 = rt % 12;
    UNR for (int q = 0; q < 3; ++q) glds(lb + q * 4096 + w * 1024, vo, r2, w2row(q));
    {
      int i = r12;
#pragma unroll 1
      for (int n = 0; n < 12; ++n) { glds(lb + (3 + i) * 4096 + w * 1024, vo, r2, w2row(3 + i)); i = i == 11 ? 0 : i + 1; }
    }
    vmwait<12>();
    ST(10);
    bar();
    {
      int i = r12;
#pragma unroll 1
      for (int n = 0; n < 12; ++n) { glds(lb + 65536 + i * 4096 + w * 1024, vo, r2, w2row(15 + i)); i = i == 11 ? 0 : i + 1; }
    }
    vmwait<12>();
    ST(11);
    bar();
    UNR for (int j = 0; j < 16; ++j) {
      const int q = (j + rt) & 15;
      const int base = q < 12 ? 114688 + q * 4096 : q == 12 ? 61440 : (q - 13) * 4096;
      PIN(st1[j]);
      *(LAS v4u*)(L + base + w * 1024 + l16) = st1[j];
    }
    vmwait<0>();
    ST(12);
    bar();
    ST_OUT();
  }
}

extern "C" void kernel_launch(void* const* d_in, const int* in_sizes, int n_in, void* d_out, int out_size, void* d_ws, size_t ws_size,
                              hipStream_t stream) {
  Params p;
  p.x = (const u16*)d_in[0]; p.wqkv = (const u16*)d_in[1]; p.wphi = (const u16*)d_in[2]; p.wo = (const u16*)d_in[3];
  p.w1 = (const u16*)d_in[5]; p.b1 = (const u16*)d_in[6]; p.w2 = (const u16*)d_in[7]; p.b2 = (const u16*)d_in[8];
  p.out = (u16*)d_out; p.ws = (char*)d_ws;
  fused_block<<<256, 512, 0, stream>>>(p);
}
```

```cpp
#include <hip/hip_runtime.h>
#include <hip/hip_bf16.h>
#pragma clang diagnostic ignored "-Warray-bounds"

typedef unsigned short u16;
typedef unsigned u32;
typedef unsigned long long u64;
typedef u32 v4u __attribute__((ext_vector_type(4)));
typedef u32 v2u __attribute__((ext_vector_type(2)));
typedef float f32x2 __attribute__((ext_vector_type(2)));
typedef float f32x4 __attribute__((ext_vector_type(4)));
typedef float f32x16 __attribute__((ext_vector_type(16)));
typedef __bf16 bf16x2 __attribute__((ext_vector_type(2)));
typedef __bf16 bf16x8 __attribute__((ext_vector_type(8)));
typedef short s16x4 __attribute__((ext_vector_type(4)));
typedef __amdgpu_buffer_rsrc_t rsrc_t;
#define DI __device__ __forceinline__
#define LAS __attribute__((address_space(3)))
#define UNR _Pragma("unroll")
#define SB() __builtin_amdgcn_sched_barrier(0)

constexpr int OFF_E = 0, OFF_F1 = 4096, OFF_F3 = OFF_F1 + 32768, OFF_F2 = OFF_F3 + 32768, OFF_KVP = OFF_F2 + 65536;
constexpr int OFF_HS = 10752 << 10;
constexpr int OFF_O = 1 << 20, OFF_H = 3 << 20, OFF_HID = 5 << 20, WS_BYTES = 11 << 20;
constexpr int L_WKV = 0, L_WQ = 65536, L_XR = 98304, L_CSP = L_XR + 16384 + 2048;
constexpr int L_CSQ = L_XR + 32768, L_WO = 0, L_CS1 = 57344, L_AUG = 61440, L_STAT = 64512;
constexpr int LDS_BYTES = 163840;
constexpr int w1_off(int j, int ks) { return 65536 + (ks >> 2) * 12288 + j * 4096 + (ks & 3) * 1024; }
constexpr int w2_base(int p) { return p < 15 ? p * 4096 : p < 31 ? 65536 + ((p - 15) >> 1) * 12288 + ((p - 15) & 1) * 4096 : p < 39 ? 73728 + (p - 31) * 12288 : p == 39 ? 61440 : (p - 40) * 4096; }
constexpr int ubase(int m) { return m < 11 ? 3 * m : 2 * m + 11; }
constexpr int SC1 = 16;
constexpr u32 SPIN_CAP = 1u << 16;
constexpr u64 TAG_STEP = 0x9E3779B97F4A7C15ull;

DI f32x16 mfma32(v4u a, v4u b, f32x16 c) { return __builtin_amdgcn_mfma_f32_32x32x16_bf16(__builtin_bit_cast(bf16x8, a), __builtin_bit_cast(bf16x8, b), c, 0, 0, 0); }
DI f32x4 mfma16(v4u a, v4u b, f32x4 c) { return __builtin_amdgcn_mfma_f32_16x16x32_bf16(__builtin_bit_cast(bf16x8, a), __builtin_bit_cast(bf16x8, b), c, 0, 0, 0); }
typedef short s16x4v __attribute__((ext_vector_type(4)));
DI f32x4 sum8(v4u f, u32 ones, f32x4 c) {
  const s16x4v o = __builtin_bit_cast(s16x4v, (v2u){ones, ones});
  c = __builtin_amdgcn_mfma_f32_4x4x4bf16_1k(o, __builtin_bit_cast(s16x4v, (v2u){f.x, f.y}), c, 0, 0, 0);
  return __builtin_amdgcn_mfma_f32_4x4x4bf16_1k(o, __builtin_bit_cast(s16x4v, (v2u){f.z, f.w}), c, 0, 0, 0);
}
DI f32x4 gram4(v2u f, f32x4 c) { const s16x4v a = __builtin_bit_cast(s16x4v, f); return __builtin_amdgcn_mfma_f32_4x4x4bf16_1k(a, a, c, 0, 0, 0); }
DI u32 pk(float a, float b) { f32x2 f = {a, b}; return __builtin_bit_cast(u32, __builtin_convertvector(f, bf16x2)); }
DI u32 pk2(f32x2 f) { return __builtin_bit_cast(u32, __builtin_convertvector(f, bf16x2)); }
DI float lo(u32 x) { return __builtin_bit_cast(float, x << 16); }
DI float hi(u32 x) { return __builtin_bit_cast(float, x & 0xffff0000u); }
DI float dot2(u32 a, u32 b, float c) { return __builtin_amdgcn_fdot2_f32_bf16(__builtin_bit_cast(bf16x2, a), __builtin_bit_cast(bf16x2, b), c, false); }
constexpr u32 ONES = 0x3f803f80u;
DI v2u tr8(LAS char* p) { return __builtin_bit_cast(v2u, __builtin_amdgcn_ds_read_tr16_b64_v4i16((LAS s16x4*)p)); }
DI v4u trfrag(LAS char* p, int second) { v2u a = tr8(p), b = tr8(p + second); return (v4u){a.x, a.y, b.x, b.y}; }
DI v4u lds128(LAS char* p) { return *(LAS v4u*)p; }
DI void bar() { SB(); asm volatile("s_waitcnt lgkmcnt(0)\n\ts_barrier" ::: "memory"); SB(); }
DI void bar_nw() { SB(); asm volatile("s_barrier" ::: "memory"); SB(); }
#define PAD(n, next) do { SB(); asm volatile("s_branch .Lpag" #n "\n\t.p2align 12\n.Lpad" #n ":\n\ts_cbranch_execz .Lpad" #next "\n.Lpag" #n ":"); SB(); } while (0)
#define PIN(x) asm volatile("" : "+v"(x))
DI void pinf(float& x) { PIN(x); }
DI void pinu(u32& x) { PIN(x); }
DI void pin4(f32x4& x) { PIN(x); }
DI void pin16(f32x16& x) { PIN(x); }
DI void pinv(v4u& x) { PIN(x); }
DI u32 here(u32 x) { PIN(x); return x; }
template <int N> DI void vmwait() { asm volatile("s_waitcnt vmcnt(%0)" ::"i"(N) : "memory"); }
DI void glds(u32 lds_addr, u32 voff, rsrc_t rs, u32 soff) {
  asm volatile("s_mov_b32 m0, %0\n\ts_nop 0\n\tbuffer_load_dwordx4 %1, %2, %3 offen lds" ::"s"(lds_addr), "v"(voff), "s"(rs), "s"(soff) : "memory");
}
DI void glds2(u32 lds_addr, int ofs, u32 voff, rsrc_t rs, u32 soff) {
  asm volatile("s_add_u32 m0, %0, %4\n\ts_nop 0\n\tbuffer_load_dwordx4 %1, %2, %3 offen lds" ::"s"(lds_addr), "v"(voff), "s"(rs), "s"(soff), "i"(ofs) : "memory", "scc");
}
template <int OFS> DI void glds_o(u32 voff, rsrc_t rs, u32 soff) {
  asm volatile("buffer_load_dwordx4 %0, %1, %2 offen offset:%3 lds" ::"v"(voff), "s"(rs), "s"(soff), "i"(OFS) : "memory");
}
DI LAS char* opq(LAS char* p) { u32 a = (u32)(size_t)p; asm volatile("" : "+v"(a)); return (LAS char*)(size_t)a; }
DI void lda(v4u& d, u32 voff, rsrc_t rs, u32 soff) { asm volatile("buffer_load_dwordx4 %0, %1, %2, %3 offen" : "=v"(d) : "v"(voff), "s"(rs), "s"(soff) : "memory"); }
DI void lda_sc1(v4u& d, u32 voff, rsrc_t rs, u32 soff) { asm volatile("buffer_load_dwordx4 %0, %1, %2, %3 offen sc1" : "=v"(d) : "v"(voff), "s"(rs), "s"(soff) : "memory"); }
DI v4u ldg(rsrc_t rs, u32 voff, u32 soff) { return __builtin_amdgcn_raw_buffer_load_b128(rs, voff, soff, SC1); }
DI void stg(v4u v, rsrc_t rs, u32 voff, u32 soff) { __builtin_amdgcn_raw_buffer_store_b128(v, rs, voff + soff, 0, SC1); }
DI u32 umax3(u32 a, u32 b, u32 c) { return max(max(a, b), c); }
DI u32 mx4(u32 m, v4u d) { return umax3(umax3(m, d.x, d.y), d.z, d.w); }
DI bool anybad(u32 m) { return __builtin_amdgcn_ballot_w64(m == 0xffffffffu) != 0ull; }
DI float fbits(u32 x) { return __builtin_bit_cast(float, x); }
DI u32 ubits(float x) { return __builtin_bit_cast(u32, x); }
DI float fmx(float a, float b) { return __builtin_elementwise_maximum(a, b); }
DI float fmn(float a, float b) { return __builtin_elementwise_minimum(a, b); }
DI float xadd32(float a) { auto r = __builtin_amdgcn_permlane32_swap(ubits(a), ubits(a), false, false); return fbits(r[0]) + fbits(r[1]); }
DI float xadd16(float a) { auto r = __builtin_amdgcn_permlane16_swap(ubits(a), ubits(a), false, false); return fbits(r[0]) + fbits(r[1]); }
template <int CTRL> DI float dppadd(float a) { return a + fbits(__builtin_amdgcn_update_dpp(0u, ubits(a), CTRL, 0xf, 0xf, true)); }
DI v4u pack8(const f32x16& a, int o) { return (v4u){pk(a[o], a[o + 1]), pk(a[o + 2], a[o + 3]), pk(a[o + 4], a[o + 5]), pk(a[o + 6], a[o + 7])}; }
DI f32x16 zero16() { f32x16 z; UNR for (int i = 0; i < 16; ++i) z[i] = 0.f; return z; }
DI v4u aug_a(float cs, u32 bias_bits) {
  const u32 h2 = pk(cs, cs); const float ch = lo(h2);
  const u32 l2 = pk(cs - ch, 0.f);
  return (v4u){(h2 & 0xffffu) | (l2 << 16), (h2 & 0xffffu) | (bias_bits << 16), bias_bits, 0u};
}
DI void aug_a0(v4u& r, float cs) {
  r.y = pk(cs, 0.f);
  r.x = pk(cs, cs - lo(r.y));
}
DI void gelu_arg(const f32x16& a, f32x2 (&g)[8], float c1, float c2) {
  UNR for (int j = 0; j < 8; ++j) { const f32x2 v = {a[2 * j], a[2 * j + 1]}; g[j] = (v * v * c2 + c1) * v; PIN(g[j]); }
}
template <class F> DI v4u gelu8(const f32x16& a, int o, float c1, float c2, float sd, F hook) {
  f32x2 g[4];
  UNR for (int j = 0; j < 4; ++j) { const f32x2 v = {a[o + 2 * j], a[o + 2 * j + 1]}; g[j] = (v * v * c2 + c1) * v; }
  SB(); hook(0); SB();
  UNR for (int j = 0; j < 4; ++j) g[j] = (f32x2){__builtin_amdgcn_exp2f(g[j].x), __builtin_amdgcn_exp2f(g[j].y)};
  SB(); hook(1); SB();
  UNR for (int j = 0; j < 4; ++j) g[j] = g[j] * sd + sd;
  SB();
  UNR for (int j = 0; j < 4; ++j) g[j] = (f32x2){__builtin_amdgcn_rcpf(g[j].x), __builtin_amdgcn_rcpf(g[j].y)};
  SB(); hook(2); SB();
  UNR for (int j = 0; j < 4; ++j) g[j] *= (f32x2){a[o + 2 * j], a[o + 2 * j + 1]};
  SB();
  return (v4u){pk2(g[0]), pk2(g[1]), pk2(g[2]), pk2(g[3])};
}

struct Params {
  const u16 *x, *wqkv, *wphi;
  char* ws;
  const u16 *wo, *w1, *b1, *w2;
  const u16* b2;
  u16* out;
};

template <int LO, int HI, int R, class LD> DI void load_fr(v4u (&f)[R], LD ld) {
  UNR for (int i = LO; i < HI; ++i) { f[i % R] = ld(i); SB(); }
}
template <int LO, int HI, int R> DI bool bad_fr(v4u (&f)[R]) {
  u32 m = 0;
  UNR for (int i = LO; i < HI; ++i) m = mx4(m, f[i % R]);
  return anybad(m);
}
template <int LO, int HI, int R, class LD> DI void reload_bad(v4u (&f)[R], LD ld) {
  if constexpr (LO < HI) {
    constexpr int E = LO + 2 < HI ? LO + 2 : HI;
    if (bad_fr<LO, E, R>(f)) load_fr<LO, E, R>(f, ld);
    SB();
    reload_bad<E, HI, R>(f, ld);
  }
}
template <int LO, int HI, int INFL, int R, class LD> DI void ensure(v4u (&f)[R], LD ld) {
  if (__builtin_expect(bad_fr<LO, HI, R>(f), 0)) {
    u32 spins = 0;
    do {
      asm volatile("" ::: "memory");
      load_fr<LO, HI, R>(f, ld);
      reload_bad<HI, INFL, R>(f, ld);
      __builtin_amdgcn_s_waitcnt(0x0F70);
    } while (bad_fr<LO, HI, R>(f) && ++spins < SPIN_CAP);
    SB();
  }
}
template <int G, int S, int N, int R> DI void chk(v4u (&f)[R], u32& m, u64& bad) {
  if constexpr (S + G < N) {
    m = mx4(S % G ? m : 0u, f[(S + G) % R]);
    if constexpr (S % G == G - 1 || S + G + 1 == N) { bad = __builtin_amdgcn_ballot_w64(m == 0xffffffffu); asm volatile("" : "+s"(bad)); } else PIN(m);
  }
}
template <int G, int S, int N, int INFL, int R, class LD> DI void fix(v4u (&f)[R], u64 bad, LD ld) {
  if constexpr (S + G < N && (S % G == G - 1 || S + G + 1 == N)) {
    constexpr int LO = (S + G) / G * G, HI = S + G + 1, RE = HI + 4 < INFL ? HI + 4 : INFL;
    if (__builtin_expect(bad != 0ull, 0)) {
      u32 spins = 0;
      do {
        asm volatile("" ::: "memory");
        if constexpr (R == 16) { load_fr<LO, HI, R>(f, ld); reload_bad<HI, INFL, R>(f, ld); } else load_fr<LO, RE, R>(f, ld);
        __builtin_amdgcn_s_waitcnt(0x0F70);
      } while (bad_fr<LO, HI, R>(f) && ++spins < SPIN_CAP);
    }
  }
}
template <int VM, int LG> DI void wait2() { __builtin_amdgcn_s_waitcnt((VM & 15) | ((VM >> 4) << 14) | 0x70 | (LG << 8)); SB(); }
template <int I, int N, class F> DI void sfor(F&& f) { if constexpr (I < N) { f(std::integral_constant<int, I>{}); sfor<I + 1, N>(f); } }
constexpr int cmin(int a, int b) { return a < b ? a : b; }
constexpr int cmax(int a, int b) { return a > b ? a : b; }
constexpr int CODE_4K = 11;
constexpr int WO1 = 10, QB0 = 3, QB15 = 12, QB16 = 31, NST = 9;
constexpr int PRE = 12, L4 = 13, G4 = 4, LB = 10;
constexpr int n4(int s) { return cmin(cmax(cmin(PRE + 2 * s, s + L4), PRE), 43); }
constexpr int LA = 8;

__global__ void __launch_bounds__(512, 2) __attribute__((aligned(4096))) fused_block(Params p) {
  __shared__ __attribute__((aligned(1024))) char smem[LDS_BYTES];
  LAS char* L = (LAS char*)smem;
  const int lane = threadIdx.x & 63, wv = __builtin_amdgcn_readfirstlane(threadIdx.x >> 6), w = wv & 3;
  const int b = blockIdx.x, xc = b & 7, ib = b >> 3;
  u32 cpf = 0;
  if (wv == 4 && ib < CODE_4K) {
    u64 pc0;
    asm volatile("s_getpc_b64 %0" : "=s"(pc0));
    asm volatile("global_load_dword %0, %1, %2" : "+v"(cpf) : "v"(lane * 64), "s"(pc0 + ib * 4096));
  }
  SB();
  const int rb1 = (xc >> 1) * 4 + (ib & 3), hp = (xc & 1) * 8 + (ib >> 2);
  const int rb = xc * 2 + (ib >> 4), m = ib & 15, th = m & 1, cg = m >> 1;
  const int m3 = (m + 6) & 15;
  const bool three = m3 < 11;
  const int rh = (m3 & 1) * 4, l_xres = 65536 + (7 - rh) * 12288;
  const int h = lane >> 5, t = lane & 31;
  const u32 l16 = lane * 16;
  const int q4 = (lane & 15) >> 2, lsub = ((lane >> 4) & 1) * 32 + (lane & 3) * 8;
#define WS_SETUP() \
  rsrc_t rws = __builtin_amdgcn_make_buffer_rsrc(p.ws, 0, WS_BYTES, 0x00020000); asm volatile("" : "+s"(rws)); \
  u32 kvp_base = OFF_KVP + (hp * 4 + w) * 16 * 640; \
  const int tt = w & 1, kh = w >> 1; \
  u32 o_off = OFF_O + ((rb1 * 4 + w) * 32 + hp * 2) * 1024; \
  u32 h_off = OFF_H + ((rb * 4 + th * 2 + tt) * 32 + cg * 4 + kh * 2) * 1024; \
  u32 hs_off = OFF_HS + ((rb * 4 + th * 2 + tt) * 16 + cg * 2 + kh) * 256; \
  u32 hid_off = OFF_HID + ((rb * 4 + w) * 86 + m3) * 1024; \
  u32 hid2 = three ? hid_off + 32768 : WS_BYTES; \
  u32 ones = ONES; asm volatile("" : "+v"(ones));
  if (wv >= 4) {
  __builtin_amdgcn_s_setprio(1);
  v4u late;
  asm volatile("s_load_dwordx4 %0, %1, 0x40\n\t"
               "s_mov_b64 exec, 0\n\ts_cmp_eq_u32 %2, 1\n\ts_cbranch_scc1 .Lpad2\n\ts_cmp_eq_u32 %2, 2\n\ts_cbranch_scc1 .Lpad4\n\ts_cmp_eq_u32 %2, 3\n\ts_cbranch_scc1 .Lpad6\n\t"
               "s_branch .Lpad0\n\t.p2align 12\n.Lpadr:\n\ts_mov_b64 exec, -1\n\ts_waitcnt lgkmcnt(0)"
               : "=&s"(late) : "s"(__builtin_amdgcn_kernarg_segment_ptr()), "s"(w) : "scc", "memory");
  WS_SETUP();
  typedef __attribute__((address_space(1))) u16 gu16;
  const gu16* const pb2 = (const gu16*)(late.x | ((u64)late.y << 32));
  gu16* const pout = (gu16*)(late.z | ((u64)late.w << 32));
  u32 so2 = OFF_O + ((rb * 4 + th * 2 + tt) * 32 + kh * 16) * 1024, so4 = OFF_HID + ((rb * 4 + th * 2 + tt) * 86 + kh * 43) * 1024, so3[8];
  UNR for (int i = 0; i < 8; ++i) { so3[i] = OFF_H + (rb * 4 + w) * 32768 + ((i + rh) & 7) * 4096; asm volatile("" : "+s"(so3[i])); }
  asm volatile("" : "+s"(kvp_base), "+s"(o_off), "+s"(h_off), "+s"(hs_off), "+s"(hid_off), "+s"(hid2), "+s"(so2), "+s"(so4));
  v4u xf[32];
  float rstd;
  v4u baug, wpq, agq, fq0[4];
  LAS char* wq;
  {
    LAS char* const wn = opq(L + L_WKV + h * 512 + q4 * 64 + lsub);
    f32x16 ak = zero16(), av = zero16();
    f32x4 ssum = {0.f, 0.f, 0.f, 0.f}, ssq = ssum, ssr = ssum;
    const u32 sl = (((lane >> 2) ^ (lane >> 4)) & 1) ? 0u : ONES;
    v4u sel = {sl, sl, sl, sl}; PIN(sel);
    LAS char* xa[4];
    UNR for (int ks = 0; ks < 4; ++ks) xa[ks] = opq(L + L_XR + w * 4096 + t * 128 + (((2 * ks + h) ^ ((t >> 1) & 7)) * 16));
    LAS char* const csp = opq(L + L_CSP + lane * 4);
    u32 kvo = kvp_base + rb1 * 640 + (t < 16 ? (h * 16 + t) * 16 : t < 20 ? 512 + (h * 4 + t - 16) * 16 : WS_BYTES);
    PIN(kvo);
    auto st_sum = [&](int s) { ssum = mfma16(sel, xf[s], ssum); };
    auto st_sq = [&](int s) { ssq = gram4((v2u){xf[s].x, xf[s].y}, ssq); ssr = gram4((v2u){xf[s].z, xf[s].w}, ssr); };
    v4u fk[32], fv[32], wz0, wz1;
    v4u agk = {0u, 0u, 0u, 0u}; baug = agk;
    PIN(agk); PIN(baug);
    float ckp[4], cvp[4];
    auto step = [&](int s) {
      ak = mfma32(fk[s], xf[s], ak); pin16(ak);
      SB();
      fk[s + 4] = trfrag(wn + (s + 4) * 2048, 256);
      SB();
      av = mfma32(fv[s], xf[s], av); pin16(av);
      SB();
      fv[s + 4] = trfrag(wn + (s + 4) * 2048 + 1024, 256);
      SB();
    };
    UNR for (int c = 0; c < 8; ++c) {
      if (c == 3) { vmwait<0>(); asm volatile("" ::"v"(cpf)); }
      if (c == 7) PAD(0, 1);
      bar_nw();
      UNR for (int ks = 0; ks < 4; ++ks) {
        xf[c * 4 + ks] = lds128(xa[ks] + (c & 3) * 16384);
        if (c == 0) { fk[ks] = trfrag(wn + ks * 2048, 256); fv[ks] = trfrag(wn + ks * 2048 + 1024, 256); }
      }
      if (c == 0 && wv >= 6) asm volatile("global_load_dword %0, %1, %2" : "+v"(cpf) : "v"(min(lane, wv == 6 ? 21 : 7) * 128), "s"(wv == 6 ? (const gu16*)p.b1 : pb2));
      if (c == 1) {
        wpq = trfrag(L + L_WQ + ((lane >> 4) & 1) * 512 + (h * 4 + q4) * 32 + (lane & 3) * 8, 256);
        wz0 = trfrag(L + L_WQ + (lane & 1) * 512 + (h * 4 + q4) * 32 + ((lane >> 4) & 1) * 8 + (lane & 2) * 8, 256);
      }
      if (c == 7) UNR for (int i = 0; i < 4; ++i) ckp[i] = *(LAS float*)(csp + i * 4096);
      SB();
      if (c > 0) {
        UNR for (int ks = 0; ks < 4; ++ks) { st_sum(c * 4 - 4 + ks); st_sq(c * 4 - 4 + ks); SB(); }
        PIN(ssum); PIN(ssq); PIN(ssr);
      }
      SB();
      if (c < 7) UNR for (int s = c * 4; s < c * 4 + 4; ++s) step(s);
    }
    UNR for (int ks = 28; ks < 32; ++ks) st_sum(ks);
    SB();
    ak = mfma32(fk[28], xf[28], ak);
    float cks = ckp[0] + ckp[1]; PIN(cks); cks += ckp[2]; PIN(cks); cks += ckp[3]; PIN(cks);
    SB();
    ak = mfma32(fk[29], xf[29], ak);
    aug_a0(agk, cks); PIN(agk);
    UNR for (int i = 0; i < 4; ++i) cvp[i] = *(LAS float*)(csp + i * 4096 + 256);
    SB();
    ak = mfma32(fk[30], xf[30], ak);
    float nmean = ssum[0] * (-1.f / 512.f); PIN(nmean);
    baug.x = pk(nmean, nmean); baug.y = pk(nmean - lo(baug.x), 0.f); PIN(baug);
    SB();
    ak = mfma32(fk[31], xf[31], ak);
    UNR for (int k = 0; k < 4; ++k) wz1[k] = (lane & 4) ? wz0[k] : 0u;
    PIN(wz1);
    SB();
    ak = mfma32(agk, baug, ak); PIN(ak);
    UNR for (int k = 0; k < 4; ++k) wz0[k] = (lane & 4) ? 0u : wz0[k];
    PIN(wz0);
    SB();
    UNR for (int ks = 28; ks < 32; ++ks) { st_sq(ks); SB(); }
    PIN(ssq); PIN(ssr);
    SB();
    av = mfma32(fv[28], xf[28], av); PIN(av);
    LAS char* const img = opq(L + L_XR + w * 4096 + h * 2048 + t * 32);
    LAS char* const imv = opq(L + L_XR + 16384 + w * 4096 + t * 32 + h * 16);
    v4u kb0 = pack8(ak, 0); PIN(kb0);
    SB();
    f32x16 pp = mfma32(wz0, kb0, zero16());
    v4u kb1 = pack8(ak, 8); PIN(kb1);
    float cvs = cvp[0] + cvp[1]; PIN(cvs);
    SB();
    pp = mfma32(wz1, kb1, pp);
    float msq = (lane & 1) ? ssq[1] : ssq[0], msq2 = (lane & 1) ? ssq[3] : ssq[2], msr = (lane & 1) ? ssr[1] : ssr[0], msr2 = (lane & 1) ? ssr[3] : ssr[2];
    msq = ((lane & 2) ? msq2 : msq) + ((lane & 2) ? msr2 : msr); PIN(msq);
    SB();
    av = mfma32(fv[29], xf[29], av); PIN(av);
    msq = xadd32(msq) * (1.f / 512.f); PIN(msq);
    rstd = __builtin_amdgcn_rsqf(__builtin_fmaxf(__builtin_fmaf(-nmean, nmean, msq), 0.f) + 1e-5f); PIN(rstd);
    SB();
    av = mfma32(fv[30], xf[30], av); PIN(av);
    cvs += cvp[2]; PIN(cvs); cvs += cvp[3]; PIN(cvs);
    float mx = fmx(pp[0], pp[1]), mn = fmn(pp[0], pp[1]);
    UNR for (int j = 2; j < 8; ++j) { mx = fmx(mx, pp[j]); mn = fmn(mn, pp[j]); }
    PIN(mx); PIN(mn);
    SB();
    av = mfma32(fv[31], xf[31], av); PIN(av);
    aug_a0(agk, cvs); PIN(agk);
    UNR for (int j = 8; j < 14; ++j) { mx = fmx(mx, pp[j]); mn = fmn(mn, pp[j]); }
    PIN(mx); PIN(mn);
    SB();
    av = mfma32(agk, baug, av); PIN(av);
    UNR for (int j = 14; j < 16; ++j) { mx = fmx(mx, pp[j]); mn = fmn(mn, pp[j]); }
    PIN(mx); PIN(mn);
    LAS char* const pa = opq(L + L_XR + w * 1024 + lane * 8);
    LAS char* const pv = opq(L + L_XR + 16384 + (w >> 1) * 1024 + lane * 8);
    wq = opq(L + L_WQ + h * 512 + q4 * 64 + lsub);
    v4u one4;
    UNR for (int k = 0; k < 4; ++k) { u32 o = ONES; PIN(o); one4[k] = o; }
    PIN(one4);
    SB();
    {
      const float c = rstd * 1.44269504f, cmx = -c * mx, cmn = c * mn;
      f32x2 e[8], n[8];
      UNR for (int j = 0; j < 8; ++j) { e[j] = (f32x2){pp[2 * j], pp[2 * j + 1]} * c + cmx; PIN(e[j]); }
      SB();
      UNR for (int j = 0; j < 8; ++j) {
        e[j].x = __builtin_amdgcn_exp2f(e[j].x); e[j].y = __builtin_amdgcn_exp2f(e[j].y);
        n[j] = (f32x2){pp[2 * j], pp[2 * j + 1]} * (-c) + cmn; PIN(e[j]); PIN(n[j]);
        SB();
      }
      f32x2 s0, s1, s2, s3; float rp;
      UNR for (int j = 0; j < 8; ++j) {
        n[j].x = __builtin_amdgcn_exp2f(n[j].x);
        if (j == 0) s0 = e[0] + e[1];
        if (j == 1) s2 = e[4] + e[5];
        if (j == 2) s0 += s1;
        if (j == 3) s0 += s2;
        if (j == 4) rp = __builtin_amdgcn_rcpf(rp);
        if (j >= 5) e[2 * j - 10] *= rp;
        SB();
        n[j].y = __builtin_amdgcn_exp2f(n[j].y);
        if (j == 0) s1 = e[2] + e[3];
        if (j == 1) s3 = e[6] + e[7];
        if (j == 2) s2 += s3;
        if (j == 3) rp = s0.x + s0.y;
        if (j >= 5) e[2 * j - 9] *= rp;
        SB();
      }
      e[6] *= rp; e[7] *= rp;
      s0 = n[0] + n[1]; s1 = n[2] + n[3]; s2 = n[4] + n[5]; s3 = n[6] + n[7];
      SB();
      v4u o0, o1;
      o0.x = pk2(e[0]); s0 += s1; o0.y = pk2(e[1]); s2 += s3; SB();
      o0.z = pk2(e[2]); o0.w = pk2(e[3]); s0 += s2; SB();
      o1.x = pk2(e[4]); o1.y = pk2(e[5]); rp = s0.x + s0.y; SB();
      *(LAS v4u*)(img) = o0;
      o1.z = pk2(e[6]); o1.w = pk2(e[7]); rp = __builtin_amdgcn_rcpf(rp); SB();
      *(LAS v4u*)(img + 16) = o1;
      av *= rstd; PIN(av);
      SB();
      UNR for (int j = 0; j < 8; ++j) { n[j] *= rp; PIN(n[j]); }
      SB();
      *(LAS v4u*)(imv) = pack8(av, 0);
      *(LAS v4u*)(imv + 1024) = pack8(av, 8);
      SB();
      *(LAS v4u*)(img + 1024) = (v4u){pk2(n[0]), pk2(n[1]), pk2(n[2]), pk2(n[3])};
      *(LAS v4u*)(img + 1024 + 16) = (v4u){pk2(n[4]), pk2(n[5]), pk2(n[6]), pk2(n[7])};
      SB();
    }
    bar();
    f32x4 kv = {0.f, 0.f, 0.f, 0.f}, zz = {0.f, 0.f, 0.f, 0.f};
    v4u ka[4], kb[4];
    UNR for (int ww = 0; ww < 4; ++ww) { ka[ww] = trfrag(pa + ww * 4096, 512); kb[ww] = trfrag(pv + ww * 4096, 512); SB(); }
    UNR for (int ww = 0; ww < 4; ++ww) { kv = mfma16(ka[ww], kb[ww], kv); zz = mfma16(ka[ww], one4, zz); }
    SB();
    UNR for (int ks = 0; ks < 4; ++ks) fq0[ks] = trfrag(wq + ks * 1024, 256);
    SB();
    const auto s0 = __builtin_amdgcn_permlane16_swap(pk(kv[0], kv[1]), pk(zz[0], zz[1]), false, false);
    const auto s1 = __builtin_amdgcn_permlane16_swap(pk(kv[2], kv[3]), pk(zz[2], zz[3]), false, false);
    stg((v4u){s0[0], s1[0], s0[1], s1[1]}, rws, kvo, 0);
    SB();
    agq = agk;
  }
  bar_nw();
  v4u phq[4];
  LAS char* kvz;
  LAS char* xra[4];
  u32 ovo;
  v2u bb[4];
  {
    v4u f[32], d[9], wp0, wp1, idn;
    LAS char* kvw;
    int idb; u32 ida, vg, vz;
    auto ldk = [&](int i) { return i == 0 ? ldg(rws, vz, kvp_base) : ldg(rws, vg, kvp_base + (i - 1) * 1280); };
    UNR for (int ks = 0; ks < 4; ++ks) f[ks] = fq0[ks];
    float cqp[4];
    f32x16 aq = zero16();
    SB();
    UNR for (int ks = 0; ks < 32; ++ks) {
      if (ks + 4 < 32) f[ks + 4] = trfrag(wq + (ks + 4) * 1024, 256);
      aq = mfma32(f[ks], xf[ks], aq);
      if (ks == 1) { const u32 x = here(l16); vg = (x & 240) + ((x >> 1) & 256); PIN(vg); }
      if (ks == 2) { const u32 x = here(l16); vg += ((x >> 8) & 1) * 640; PIN(vg); }
      if (ks == 3) { const u32 x = here(l16); vz = (x & 112) + ((x >> 1) & 128); PIN(vz); }
      if (ks == 4) { const u32 x = here(l16); vz = vz * 40 + 512 + ((x >> 3) & 64); PIN(vz); }
      if (ks == 5) { const u32 x = here(l16); idb = ((x >> 5) & 7) - ((x >> 7) & 4); PIN(idb); }
      if (ks == 6) { const u32 x = here(l16); ida = 0x3f80u << (x & 16); PIN(ida); }
      if (ks == 7) kvz = opq(L + l_xres + 8192 + here(l16));
      if (ks == 24) { bar_nw(); UNR for (int i = 0; i < 4; ++i) cqp[i] = *(LAS float*)(L + L_CSQ + i * 256 + lane * 4); }
      if (ks == 27) { cqp[0] += cqp[1]; PIN(cqp[0]); cqp[0] += cqp[2]; PIN(cqp[0]); cqp[0] += cqp[3]; PIN(cqp[0]); }
      if (ks == 28) { aug_a0(agq, cqp[0]); PIN(agq); }
      if (ks == 29) aq = mfma32(agq, baug, aq);
      if (ks >= 8 && ks < 12) { wp0[ks - 8] = (lane & 16) ? 0u : wpq[ks - 8]; wp1[ks - 8] = (lane & 16) ? wpq[ks - 8] : 0u; }
      if (ks == 12) { PIN(wp0); PIN(wp1); }
      if (ks >= 13 && ks < 17) { const int k = ks - 13; u32 v = idb == k ? ida : 0u; PIN(v); idn[k] = v; }
      if (ks == 17) PIN(idn);
      if (ks >= 18 && ks < 22) xra[ks - 18] = opq(L + l_xres + (tt * 32 + t) * 128 + (((kh * 4 + ks - 18) ^ ((t >> 1) & 7)) * 16) + h * 8);
      if (ks == 22) { ovo = o_off + here(l16); PIN(ovo); }
      if (ks == 23) { const u32 x = here(l16); kvw = opq(L + l_xres + 8192 + w * 1024 + (x & 752) + ((x >> 5) & 8)); }
      if (ks >= 29) d[ks - 29] = ldk(ks - 29);
      SB();
    }
    d[3] = ldk(3);
    PAD(2, 3);
    SB();
    v4u qb0 = pack8(aq, 0); PIN(qb0);
    SB();
    f32x16 pp = mfma32(wp0, qb0, zero16());
    v4u qb1 = pack8(aq, 8); PIN(qb1);
    SB();
    pp = mfma32(wp1, qb1, pp);
    d[4] = ldk(4); d[5] = ldk(5); d[6] = ldk(6);
    SB();
    {
      const float c = rstd * 1.44269504f;
      float mx0 = fmx(pp[0], pp[1]), mn0 = fmn(pp[0], pp[1]), mx1 = fmx(pp[8], pp[9]), mn1 = fmn(pp[8], pp[9]);
      UNR for (int j = 2; j < 8; j += 2) {
        mx0 = fmx(fmx(mx0, pp[j]), pp[j + 1]); mn0 = fmn(fmn(mn0, pp[j]), pp[j + 1]);
        mx1 = fmx(fmx(mx1, pp[8 + j]), pp[9 + j]); mn1 = fmn(fmn(mn1, pp[8 + j]), pp[9 + j]);
        SB();
      }
      auto r = __builtin_amdgcn_permlane32_swap(ubits(mx0), ubits(mx1), false, false);
      auto q = __builtin_amdgcn_permlane32_swap(ubits(mn0), ubits(mn1), false, false);
      float mx = -c * fmx(fbits(r[0]), fbits(r[1])), mn = c * fmn(fbits(q[0]), fbits(q[1]));
      r = __builtin_amdgcn_permlane32_swap(ubits(mx), ubits(mx), false, false);
      q = __builtin_amdgcn_permlane32_swap(ubits(mn), ubits(mn), false, false);
      SB();
      f32x2 e[8], n[8];
      UNR for (int j = 0; j < 8; ++j) { e[j] = (f32x2){pp[2 * j], pp[2 * j + 1]} * c + fbits(r[j >> 2]); PIN(e[j]); }
      SB();
      UNR for (int j = 0; j < 8; ++j) {
        e[j].x = __builtin_amdgcn_exp2f(e[j].x); e[j].y = __builtin_amdgcn_exp2f(e[j].y);
        n[j] = (f32x2){pp[2 * j], pp[2 * j + 1]} * (-c) + fbits(q[j >> 2]); PIN(e[j]); PIN(n[j]);
        SB();
      }
      f32x2 s0, s1, s2, s3; float p0, p1;
      UNR for (int j = 0; j < 8; ++j) {
        n[j].x = __builtin_amdgcn_exp2f(n[j].x);
        if (j == 0) s0 = e[0] + e[1];
        if (j == 1) s2 = e[4] + e[5];
        if (j == 2) s0 += s1;
        if (j == 3) p0 = s0.x + s0.y;
        if (j == 4) r = __builtin_amdgcn_permlane32_swap(ubits(p0), ubits(p1), false, false);
        if (j == 5) p0 = __builtin_amdgcn_rcpf(p0);
        if (j == 6) r = __builtin_amdgcn_permlane32_swap(ubits(p0), ubits(p0), false, false);
        SB();
        n[j].y = __builtin_amdgcn_exp2f(n[j].y);
        if (j == 0) s1 = e[2] + e[3];
        if (j == 1) s3 = e[6] + e[7];
        if (j == 2) s2 += s3;
        if (j == 3) p1 = s2.x + s2.y;
        if (j == 4) p0 = fbits(r[0]) + fbits(r[1]);
        if (j >= 6) d[j + 1] = ldk(j + 1);
        SB();
      }
      bar_nw();
      s0 = n[0] + n[1]; s1 = n[2] + n[3]; s2 = n[4] + n[5]; s3 = n[6] + n[7];
      SB();
      e[0] *= fbits(r[0]); s0 += s1; e[1] *= fbits(r[0]); s2 += s3; SB();
      e[2] *= fbits(r[0]); p0 = s0.x + s0.y; e[3] *= fbits(r[0]); p1 = s2.x + s2.y; SB();
      e[4] *= fbits(r[1]); e[5] *= fbits(r[1]); SB();
      q = __builtin_amdgcn_permlane32_swap(ubits(p0), ubits(p1), false, false);
      e[6] *= fbits(r[1]); e[7] *= fbits(r[1]); SB();
      p0 = fbits(q[0]) + fbits(q[1]);
      phq[0].x = pk2(e[0]); phq[0].y = pk2(e[1]); SB();
      p0 = __builtin_amdgcn_rcpf(p0);
      phq[0].z = pk2(e[2]); phq[0].w = pk2(e[3]); phq[2].x = pk2(e[4]); phq[2].y = pk2(e[5]); SB();
      q = __builtin_amdgcn_permlane32_swap(ubits(p0), ubits(p0), false, false);
      phq[2].z = pk2(e[6]); phq[2].w = pk2(e[7]); SB();
      UNR for (int j = 0; j < 8; ++j) { n[j] *= fbits(q[j >> 2]); PIN(n[j]); }
      SB();
      phq[1] = (v4u){pk2(n[0]), pk2(n[1]), pk2(n[2]), pk2(n[3])};
      phq[3] = (v4u){pk2(n[4]), pk2(n[5]), pk2(n[6]), pk2(n[7])};
    }
    PIN(phq[0]); PIN(phq[1]); PIN(phq[2]); PIN(phq[3]);
    f32x4 s; float zs[4];
    auto red = [&]() {
      f32x4 z4 = mfma16(idn, d[0], (f32x4){0.f, 0.f, 0.f, 0.f}); pin4(z4);
      u32 mm = mx4(0u, d[0]); pinu(mm);
      s = (f32x4){0.f, 0.f, 0.f, 0.f};
      SB();
      UNR for (int i = 1; i < 9; ++i) {
        s = mfma16(idn, d[i], s); pin4(s);
        SB();
        mm = mx4(mm, d[i]); pinu(mm);
        if (i == 3) UNR for (int k = 0; k < 4; ++k) { zs[k] = dppadd<0x124>(z4[k]); pinf(zs[k]); }
        if (i == 4) UNR for (int k = 0; k < 4; ++k) { zs[k] = dppadd<0x122>(zs[k]); pinf(zs[k]); }
        if (i == 5) UNR for (int k = 0; k < 4; ++k) { zs[k] = dppadd<0x121>(zs[k]); pinf(zs[k]); }
        if (i == 6) *(LAS v2u*)(kvw + 256) = (v2u){pk(zs[0], zs[1]), pk(zs[2], zs[3])};
        SB();
      }
      return anybad(mm);
    };
    if (__builtin_expect(red(), 0)) {
      u32 spins = 0;
      do {
        asm volatile("" ::: "memory");
        UNR for (int i = 0; i < 9; ++i) { if (anybad(mx4(0u, d[i]))) d[i] = ldk(i); SB(); }
        __builtin_amdgcn_s_waitcnt(0x0F70);
      } while (red() && ++spins < SPIN_CAP);
    }
    *(LAS v2u*)kvw = (v2u){pk(s[0], s[1]), pk(s[2], s[3])};
  }
  bar();
  f32x16 keep;
  {
    const v4u k0 = lds128(kvz), k1 = lds128(kvz + 1024), k2 = lds128(kvz + 2048), k3 = lds128(kvz + 3072);
    SB();
    v2u xr[4];
    UNR for (int g = 0; g < 4; ++g) xr[g] = *(LAS v2u*)xra[g];
    SB();
    f32x16 a = mfma32(k0, phq[0], zero16());
    a = mfma32(k1, phq[1], a);
    f32x16 c = mfma32(k2, phq[2], zero16());
    c = mfma32(k3, phq[3], c);
    PIN(a); PIN(c);
    SB();
    float ra = __builtin_amdgcn_rcpf(a[8] + 1e-6f);
    float av[8];
    UNR for (int j = 0; j < 8; ++j) { av[j] = a[j] * ra; PIN(av[j]); }
    stg((v4u){pk(av[0], av[1]), pk(av[2], av[3]), pk(av[4], av[5]), pk(av[6], av[7])}, rws, ovo, 0);
    SB();
    c *= __builtin_amdgcn_rcpf(c[8] + 1e-6f);
    stg(pack8(c, 0), rws, ovo + 1024, 0);
    UNR for (int g = 0; g < 4; ++g) bb[g] = *(const __attribute__((address_space(1))) v2u*)(pb2 + cg * 64 + kh * 32 + g * 8 + h * 4);
    bar_nw();
    UNR for (int g = 0; g < 4; ++g) { keep[4 * g] = lo(xr[g].x); keep[4 * g + 1] = hi(xr[g].x); keep[4 * g + 2] = lo(xr[g].y); keep[4 * g + 3] = hi(xr[g].y); }
    PIN(keep);
  }
  LAS char* const w1a = opq(L + 65536 + h * 256 + q4 * 64 + lsub), * const w1b = opq(L + 114688 + h * 256 + q4 * 64 + lsub);
  auto w1f = [&](int j, int s) { return s < 16 ? trfrag(w1a + w1_off(j, s) - 65536, 512) : trfrag(w1b + w1_off(j, s) - 114688, 512); };
  {
    f32x16 send = zero16();
    LAS char* const wkeep = opq(L + L_WO + kh * 32768 + h * 1024 + q4 * 128 + ((kh ^ (q4 >> 1)) * 64) + lsub);
    LAS char* const wsend = opq(L + L_WO + kh * 32768 + h * 1024 + q4 * 128 + ((kh ^ 1 ^ (q4 >> 1)) * 64) + lsub);
    LAS char* const redw = opq(L + L_WO + tt * 4096 + kh * 32768 + l16);
    LAS char* const redr = opq(L + L_WO + tt * 4096 + (kh ^ 1) * 32768 + l16);
    u32 hvo = h_off + l16, hso = hs_off + lane * 4; PIN(hvo); PIN(hso);
    v4u wa[16], wb[16];
    UNR for (int s = 0; s < 4; ++s) { wa[s] = trfrag(wkeep + s * 2048, 512); wb[s] = trfrag(wsend + s * 2048, 512); }
    auto ld = [&](int i) { return ldg(rws, l16 + (i & 3) * 1024, so2 + (i >> 2) * 4096); };
    v4u f[16];
    PAD(4, 5);
    vmwait<0>();
    load_fr<0, LA, 16>(f, ld);
    ensure<0, 2, LA, 16>(f, ld);
    load_fr<LA, LB, 16>(f, ld);
    u32 mm; u64 bad;
    sfor<0, 14>([&](auto G) {
      constexpr int s = G.value;
      wait2<cmin(s + LB, 16) - s - 3, 4>();
      send = mfma32(wb[s], f[s], send);
      SB();
      if (s >= 2) wb[s + 2] = trfrag(wsend + (s + 2) * 2048, 512);
      SB();
      chk<2, s, 16, 16>(f, mm, bad);
      SB();
      keep = mfma32(wa[s], f[s], keep);
      SB();
      if (s >= 2) wa[s + 2] = trfrag(wkeep + (s + 2) * 2048, 512);
      SB();
      if (s + LB < 16) f[s + LB] = ld(s + LB);
      if (s + LB == 16) bar_nw();
      SB();
      fix<2, s, 16, cmin(s + LB + 1, 16), 16>(f, bad, ld);
      SB();
    });
    send = mfma32(wb[14], f[14], send); send = mfma32(wb[15], f[15], send); PIN(send);
    SB();
    keep = mfma32(wa[14], f[14], keep); keep = mfma32(wa[15], f[15], keep); PIN(keep);
    SB();
    UNR for (int g = 0; g < 4; ++g) *(LAS f32x4*)(redw + g * 1024) = (f32x4){send[4 * g], send[4 * g + 1], send[4 * g + 2], send[4 * g + 3]};
    bar();
    UNR for (int g = 0; g < 4; ++g) {
      const f32x4 r = *(LAS f32x4*)(redr + g * 1024);
      UNR for (int k = 0; k < 4; ++k) keep[4 * g + k] += r[k];
    }
    const v4u h0 = pack8(keep, 0), h1 = pack8(keep, 8);
    stg(h0, rws, hvo, 0); stg(h1, rws, hvo + 1024, 0);
    SB();
    float ss = 0.f, sq = 0.f;
    UNR for (int k = 0; k < 4; ++k) { ss = dot2(h0[k], ones, ss); sq = dot2(h0[k], h0[k], sq); ss = dot2(h1[k], ones, ss); sq = dot2(h1[k], h1[k], sq); }
    const auto sw = __builtin_amdgcn_permlane32_swap(ubits(ss), ubits(sq), false, false);
    __builtin_amdgcn_raw_buffer_store_b32(ubits(fbits(sw[0]) + fbits(sw[1])), rws, hso, 0, SC1);
    bar();
    UNR for (int g = 0; g < 4; ++g) {
      const u32 r0 = g < 2 ? h0[g * 2] : h1[g * 2 - 4], r1 = g < 2 ? h0[g * 2 + 1] : h1[g * 2 - 3];
      keep[4 * g] = lo(r0) + lo(bb[g].x); keep[4 * g + 1] = hi(r0) + hi(bb[g].x); keep[4 * g + 2] = lo(r1) + lo(bb[g].y); keep[4 * g + 3] = hi(r1) + hi(bb[g].y);
    }
    PIN(keep);
  }
  v4u wa[43], wb[43], f[20];
  auto ld4 = [&](int i) { return ldg(rws, l16 + (i & 3) * 1024, so4 + (i >> 2) * 4096); };
  const u32 w2k = kh * 2048 + h * 512 + q4 * 128 + ((kh ^ (q4 >> 1)) * 64) + lsub;
  {
    auto ld = [&](int i) { return ldg(rws, l16 + (i & 3) * 1024, so3[i >> 2]); };
    v4u ua[32], ub[32];
    PAD(1, r);
    vmwait<0>();
    load_fr<0, LA, 32>(xf, ld);
    bar_nw();
    UNR for (int s = 0; s < 2; ++s) { ua[s] = w1f(0, s); ub[s] = w1f(1, s); }
    f32x16 a0 = zero16(), a1 = zero16();
    v4u ba = {0u, 0u, 0u, 0u}, ag0, ag1, ag2; float c1, c2, sd, mu, ms;
    PIN(ba);
    ensure<0, 2, LA, 32>(xf, ld);
    load_fr<LA, LB, 32>(xf, ld);
    u32 mm; u64 bad;
    sfor<0, 30>([&](auto G) {
      constexpr int s = G.value;
      wait2<cmin(s + LB, 32) - s - 3, 4>();
      a0 = mfma32(ua[s], xf[s], a0);
      SB();
      ua[s + 2] = w1f(0, s + 2);
      SB();
      chk<2, s, 32, 32>(xf, mm, bad);
      SB();
      a1 = mfma32(ub[s], xf[s], a1);
      SB();
      ub[s + 2] = w1f(1, s + 2);
      SB();
      if (s + LB < 32) xf[s + LB] = ld(s + LB);
      if (s == 22) {
        bar_nw();
        mu = *(LAS float*)(L + L_STAT + w * 256 + t * 4); ms = *(LAS float*)(L + L_STAT + w * 256 + 128 + t * 4);
      }
      if (s == 23) ag0 = lds128(L + L_AUG + l16);
      if (s == 24) { ag1 = lds128(L + L_AUG + 1024 + l16); ag2 = lds128(L + L_AUG + 2048 + l16); }
      if (s == 25) { ms = __builtin_fmaxf(__builtin_fmaf(-mu, mu, ms), 0.f) + 1e-5f; PIN(ms); }
      if (s == 26) { c1 = __builtin_amdgcn_rsqf(ms); ba.x = pk(-mu, -mu); mu = -mu - lo(ba.x); PIN(c1); PIN(mu); }
      if (s == 27) { sd = ms * c1; ba.z = pk(sd, 0.f); PIN(sd); }
      if (s == 28) { ms = sd - lo(ba.z); ba.y = pk(mu, sd); ba.z = pk(ms, 0.f); PIN(ba); }
      SB();
      fix<2, s, 32, cmin(s + LB + 1, 32), 32>(xf, bad, ld);
      SB();
    });
    a0 = mfma32(ag0, ba, a0); PIN(a0);
    SB();
    c2 = c1 * c1; PIN(c2); c1 *= -2.302208198f; c2 *= 0.044715f; PIN(c2); c2 *= c1; PIN(c1); PIN(c2);
    SB();
    v4u uc[32];
    a0 = mfma32(ua[30], xf[30], a0); PIN(a0);
    SB();
    UNR for (int s = 0; s < 2; ++s) uc[s] = w1f(2, s);
    SB();
    a0 = mfma32(ua[31], xf[31], a0); PIN(a0);
    SB();
    UNR for (int s = 2; s < 4; ++s) uc[s] = w1f(2, s);
    u32 hv0 = hid_off + l16; PIN(hv0);
    wait2<63, 8>();
    bar_nw();
    {
      f32x2 g[8];
      gelu_arg(a0, g, c1, c2);
      SB();
      UNR for (int j = 0; j < 8; ++j) {
        if (j == 0) { a1 = mfma32(ag1, ba, a1); PIN(a1); }
        if (j == 2) { a1 = mfma32(ub[30], xf[30], a1); PIN(a1); }
        if (j == 4) { a1 = mfma32(ub[31], xf[31], a1); PIN(a1); }
        SB();
        g[j] = (f32x2){__builtin_amdgcn_exp2f(g[j].x), __builtin_amdgcn_exp2f(g[j].y)}; PIN(g[j]);
        SB();
      }
      UNR for (int j = 0; j < 8; ++j) { g[j] = g[j] * sd + sd; PIN(g[j]); }
      SB();
      UNR for (int o = 0; o < 8; o += 4) {
        UNR for (int j = o; j < o + 4; ++j) g[j] = (f32x2){__builtin_amdgcn_rcpf(g[j].x), __builtin_amdgcn_rcpf(g[j].y)};
        SB();
        UNR for (int j = o; j < o + 4; ++j) g[j] *= (f32x2){a0[2 * j], a0[2 * j + 1]};
        SB();
        stg((v4u){pk2(g[o]), pk2(g[o + 1]), pk2(g[o + 2]), pk2(g[o + 3])}, rws, hv0, (o >> 2) * 43 * 1024);
        SB();
      }
    }
    PAD(3, r);
    LAS char* const wk0 = opq(L + w2k), * const ws0 = opq(L + (w2k ^ 64));
    {
      f32x2 g[8];
      gelu_arg(a1, g, c1, c2);
      SB();
      f32x16 a2 = mfma32(ag2, ba, zero16());
      u32 hv1 = hid_off + 16384 + l16; PIN(hv1);
      float e[16]; v4u r;
      e[0] = __builtin_amdgcn_exp2f(g[0].x); PIN(e[0]);
      SB();
      sfor<0, 32>([&](auto G) {
        constexpr int s = G.value, i = s >> 1;
        if (!(s & 1) && s < 28) wait2<63, 4>();
        a2 = mfma32(uc[s], xf[s], a2);
        SB();
        if (!(s & 1)) { e[i] = __builtin_amdgcn_rcpf(__builtin_fmaf(e[i], sd, sd)); pinf(e[i]); }
        else {
          e[i] *= a1[i]; pinf(e[i]);
          if (i < 15) { e[i + 1] = __builtin_amdgcn_exp2f(g[(i + 1) >> 1][(i + 1) & 1]); pinf(e[i + 1]); }
        }
        SB();
        if (s + 4 < 32) uc[s + 4] = w1f(2, s + 4);
        else if (s < 31) { wa[s - 28] = trfrag(wk0 + (s - 28) * 4096, 1024); wb[s - 28] = trfrag(ws0 + (s - 28) * 4096, 1024); }
        SB();
        if ((s & 3) == 3) { u32 q = pk(e[i - 1], e[i]); pinu(q); r[(s >> 2) & 3] = q; }
        if ((s & 15) == 15) stg(r, rws, hv1, (s >> 4) * 43 * 1024);
        if (s >= 16 && !(s & 1)) f[(s - 16) >> 1] = ld4((s - 16) >> 1);
        SB();
      });
      bar();
      if (PRE > 8) f[8] = ld4(8);
      SB();
      stg(gelu8(a2, 0, c1, c2, sd, [&](int i) { if (9 + i < PRE) f[9 + i] = ld4(9 + i); }), rws, l16, hid2);
      SB();
      stg(gelu8(a2, 8, c1, c2, sd, [&](int i) { if (12 + i < PRE) f[12 + i] = ld4(12 + i); }), rws, l16, hid2 + 43 * 1024);
      SB();
      if (PRE > 15) f[15] = ld4(15);
      SB();
    }
  }
  {
    f32x16 send = zero16();
    LAS char* wk[3], * ws[3];
    UNR for (int i = 0; i < 3; ++i) { wk[i] = opq(L + i * 65536 + w2k); ws[i] = opq(L + i * 65536 + (w2k ^ 64)); }
    u32 lx = l16; PIN(lx);
    u32 orow = (rb * 128 + th * 64 + w * 16 + (lx >> 7)) * 1024 + cg * 128 + (lx & 112);
    PIN(orow);
    const rsrc_t rout = __builtin_amdgcn_make_buffer_rsrc((void*)pout, 0, 2048 * 512 * 2, 0x00020000);
    LAS char* const redw = opq(L + 16384 + kh * 17408 + tt * 32 * 272 + (lx & 496) * 17 + ((lx >> 5) & 16));
    LAS char* const redr = opq(L + 16384 + (w * 16 + (lx >> 7)) * 272 + (lx & 112) * 2);
    ensure<0, G4, PRE, 20>(f, ld4);
    u32 mm; u64 bad;
    sfor<0, 41>([&](auto G) {
      constexpr int s = G.value, o = w2_base(s + 2), pc = cmin(s + G4, 42);
      wait2<n4(s) - pc - 1 + (pc < 8 ? 3 : pc < cmin(12, PRE) ? 2 : pc < cmin(15, PRE) ? 1 : 0), 4>();
      send = mfma32(wb[s], f[s % 20], send);
      SB();
      if (s + 2 == QB16 - 8) bar_nw();
      if (s >= 1) wb[s + 2] = trfrag(ws[o >> 16] + (o & 65535), 1024);
      SB();
      chk<G4, s, 43, 20>(f, mm, bad);
      SB();
      keep = mfma32(wa[s], f[s % 20], keep);
      SB();
      if (s >= 1) wa[s + 2] = trfrag(wk[o >> 16] + (o & 65535), 1024);
      SB();
      load_fr<n4(s), n4(s + 1), 20>(f, ld4);
      if constexpr (s == 0) PAD(5, r);
      SB();
      fix<G4, s, 43, n4(s + 1), 20>(f, bad, ld4);
      SB();
    });
    send = mfma32(wb[41], f[1], send); send = mfma32(wb[42], f[2], send); PIN(send);
    SB();
    keep = mfma32(wa[41], f[1], keep); keep = mfma32(wa[42], f[2], keep); PIN(keep);
    SB();
    LAS char* const rws_ = redw + (kh ^ 1) * 128, * const rwk_ = redw + kh * 128;
    UNR for (int g = 0; g < 4; ++g) *(LAS f32x4*)(rws_ + g * 32) = (f32x4){send[4 * g], send[4 * g + 1], send[4 * g + 2], send[4 * g + 3]};
    SB();
    UNR for (int g = 0; g < 4; ++g) *(LAS f32x4*)(rwk_ + g * 32) = (f32x4){keep[4 * g], keep[4 * g + 1], keep[4 * g + 2], keep[4 * g + 3]};
    bar();
    f32x4 r[8];
    UNR for (int i = 0; i < 8; ++i) r[i] = *(LAS f32x4*)(redr + (i >> 2) * 8 * 272 + ((i >> 1) & 1) * 16 + (i & 1) * 17408);
    SB();
    UNR for (int g = 0; g < 2; ++g) {
      const f32x4 u = r[4 * g] + r[4 * g + 1], v = r[4 * g + 2] + r[4 * g + 3];
      __builtin_amdgcn_raw_buffer_store_b128((v4u){pk(u[0], u[1]), pk(u[2], u[3]), pk(v[0], v[1]), pk(v[2], v[3])}, rout, orow + g * 8192, 0, SC1);
    }
  }
  return;
  }
  {
    const u32 lb = (u32)(size_t)L;
    u32 vx0 = (lane >> 3) * 1024 + (((lane & 7) ^ (lane >> 4)) * 16);
    u32 vx1 = (vx0 ^ 64) + 7168, vx2 = vx0 + 14336, vx3 = vx1 + 14336;
    u32 vq = (lane >> 2) * 3072 + (lane & 3) * 16;
    const int rot = 2 * (hp + rb1); int sw = 0x80 >> hp; asm volatile("s_and_b32 %0, %0, 1" : "+s"(sw)::"scc");
    const u32 sq0 = w * 16 * 3072 + hp * 64, sx0 = (rb1 * 128 + w * 32) * 1024;
    u32 sw0 = sq0 + 1024 + ((sw + rot) & 7) * (64 * 3072), lw0 = lb + L_WKV + (sw * 4 + w) * 2048, sx00 = sx0 + (rot & 7) * 128, lx0 = lb + L_XR + w * 4096;
    asm volatile("" : "+v"(vx0), "+v"(vx1), "+v"(vx2), "+v"(vx3), "+v"(vq), "+s"(sw0), "+s"(lw0), "+s"(sx00), "+s"(lx0));
    SB();
    const rsrc_t rq = __builtin_amdgcn_make_buffer_rsrc((void*)p.wqkv, 0, 512 * 1536 * 2, 0x00020000);
    glds(lw0, vq, rq, sw0); glds_o<1024>(vq, rq, sw0);
    SB();
    const rsrc_t rx = __builtin_amdgcn_make_buffer_rsrc((void*)p.x, 0, 2048 * 512 * 2, 0x00020000);
    glds(lx0, vx0, rx, sx00); glds_o<1024>(vx1, rx, sx00); glds_o<2048>(vx2, rx, sx00); glds_o<3072>(vx3, rx, sx00);
    SB();
    auto ldw = [&](int c) {
      const u32 s = sq0 + 1024 + ((c + rot) & 7) * (64 * 3072);
      glds(lb + L_WKV + (c * 4 + w) * 2048, vq, rq, s); glds_o<1024>(vq, rq, s);
    };
    auto ldx = [&](int c) {
      const u32 s = sx0 + ((c + rot) & 7) * 128;
      glds(lb + L_XR + (c & 3) * 16384 + w * 4096, vx0, rx, s); glds_o<1024>(vx1, rx, s); glds_o<2048>(vx2, rx, s); glds_o<3072>(vx3, rx, s);
    };
    auto ldq = [&](int n) { const int c = n < 2 ? n ^ (hp & 1) : n; glds(lb + L_WQ + (c * 4 + w) * 1024, vq, rq, sq0 + ((c + rot) & 7) * (64 * 3072)); };
    if (w == 0) glds(lb + L_WQ, l16, __builtin_amdgcn_make_buffer_rsrc((void*)p.wphi, 0, 32 * 256 * 2, 0x00020000), hp * 1024);
    SB();
    WS_SETUP();
    LAS char* const wn = opq(L + h * 512 + q4 * 64 + lsub + w * 2048);
    f32x4 ck = {0.f, 0.f, 0.f, 0.f}, cv = ck;
    v4u sent = {0xffffffffu, 0xffffffffu, 0xffffffffu, 0xffffffffu}; PIN(sent);
    auto fill = [&](int i) {
      if (i == 0) stg(sent, rws, lane < 40 ? l16 + kvp_base + rb1 * 640 : lane < 56 ? l16 - 640 + hs_off : WS_BYTES, 0);
      else stg(sent, rws, l16, i < 3 ? o_off + (i - 1) * 1024 : i < 5 ? h_off + (i - 3) * 1024 : (i < 9 ? hid_off + ((i - 5) >> 1) * 16384 : hid2) + ((i - 5) & 1) * 43 * 1024);
      SB();
    };
    u64 tag;
    asm volatile("s_load_dwordx2 %0, %1, 0x0" : "=s"(tag) : "s"(p.ws + OFF_E) : "memory");
    fill(0);
    ldw(sw ^ 1);
    v4u tagv;
    u32 v8 = lane < 8 ? l16 : WS_BYTES; PIN(v8);
    auto flag = [&](u32 a0, u32 a1) { u32 a = w == 0 ? a0 : w == 1 ? a1 : WS_BYTES; asm volatile("" : "+s"(a)); stg(tagv, rws, v8, a); };
    u32 pf;
    __builtin_amdgcn_s_setprio(2);
    UNR for (int c = 0; c < 8; ++c) {
      v4u fk, fv, gk, gv;
      if (c > 0) { fk = trfrag(wn + c * 8192, 256); fv = trfrag(wn + c * 8192 + 1024, 256); }
      if (c == 1) { gk = trfrag(wn, 256); gv = trfrag(wn + 1024, 256); }
      SB();
      if (c == 7) {
        const int n = w < 2 ? 50 : w == 2 ? 19 : 22, rank = (xc & 1) ? 96 + (xc >> 1) * 32 + ib : (xc >> 1) * 24 + ib;
        const int nl = (ib < 24 || ((xc & 1) && w < 3)) ? n : 0;
        asm volatile("buffer_load_dword %0, %1, %2, 0 offen" : "=v"(pf) : "v"(lane >= nl ? 1 << 21 : w < 3 ? (rank * n + lane) * 128 : (ib * 22 + lane) * 1024 + 384),
                     "s"(__builtin_amdgcn_make_buffer_rsrc((void*)(w == 0 ? p.w1 : w == 1 ? p.w2 : p.wo), 0, w < 2 ? 43 * 32768 : 16 * 32768, 0x00020000)) : "memory");
        ck = sum8(fk, ones, ck); cv = sum8(fv, ones, cv); PIN(ck); PIN(cv);
        *(LAS float*)(L + L_CSP + w * 4096 + lane * 4) = ck[0];
        *(LAS float*)(L + L_CSP + w * 4096 + 256 + lane * 4) = cv[0];
        SB();
      }
      if (c < 7) ldx(c + 1);
      if (c < 6) ldw(c + 2);
      if (c == 6) ldq(0);
      if (c == 2) flag(OFF_F1 + (hp * 16 + rb1) * 128, WS_BYTES);
      if (c > 0 && c < 7) { ck = sum8(fk, ones, ck); cv = sum8(fv, ones, cv); }
      if (c == 1) { ck = sum8(gk, ones, ck); cv = sum8(gv, ones, cv); }
      PIN(ck); PIN(cv);
      if (c == 2) vmwait<7>(); else if (c == 6) vmwait<5>(); else if (c == 7) vmwait<2>(); else vmwait<6>();
      bar();
      if (c == 0) { asm volatile("" : "+s"(tag)); tag += TAG_STEP; tagv = (v4u){(u32)tag, (u32)(tag >> 32), (u32)tag, (u32)(tag >> 32)}; PIN(tagv); }
    }
    __builtin_amdgcn_s_setprio(0);
    auto flags_ok = [&](v4u& fl, u32 vo, u32 fo) {
#pragma unroll 1
      for (u32 spins = 0;; ++spins) {
        PIN(fl);
        if (__builtin_amdgcn_ballot_w64(fl.x != (u32)tag || fl.y != (u32)(tag >> 32)) == 0ull || spins >= SPIN_CAP) break;
        lda_sc1(fl, vo, rws, fo);
        vmwait<0>();
      }
    };
    v4u fl;
    const u32 vf4 = (w * 4 + (lane & 3)) * 128, vf8 = (w * 8 + (lane & 7)) * 128;
    lda_sc1(fl, vf4, rws, OFF_F1 + hp * 2048);
    ldq(1);
#pragma unroll 1
    for (int n = 2; n < 8; ++n) ldq(n);
    fill(1); fill(2); fill(3); fill(4);
    const rsrc_t r2 = __builtin_amdgcn_make_buffer_rsrc((void*)p.w2, 0, 1376 * 512 * 2, 0x00020000);
    const rsrc_t ro = __builtin_amdgcn_make_buffer_rsrc((void*)p.wo, 0, 512 * 512 * 2, 0x00020000);
    const rsrc_t r1 = __builtin_amdgcn_make_buffer_rsrc((void*)p.w1, 0, 512 * 1376 * 2, 0x00020000);
    const u32 vo = (lane >> 3) * 1024 + (((lane & 7) ^ ((lane >> 2) & 4)) * 16);
    const u32 s2 = w * 8192 + cg * 128;
    auto ldwo = [&](int i) { glds(lb + L_WO + (i * 4 + w) * 1024, vo, ro, i * 32768 + s2); };
    const u32 v1 = (lane >> 2) * 2752 + (lane & 3) * 16;
    const int u0 = min(3 * m3, 2 * m3 + 11);
    const u32 s10 = w * 16 * 2752 + u0 * 64;
    u32 v12 = three ? v1 : 1 << 24; PIN(v12);
    auto ldw1 = [&](int n) {
      const u32 s = s10 + n * (64 * 2752), d = lb + 65536 + ((n - rh) & 7) * 12288 + w * 1024;
      UNR for (int j = 0; j < 3; ++j) glds(d + j * 4096, j == 2 ? v12 : v1, r1, s + j * 64);
    };
    vmwait<10>();
    PIN(pf);
    flags_ok(fl, vf4, OFF_F1 + hp * 2048);
    PAD(6, 7);
    bar();
#pragma unroll 1
    for (int n = 0; n < 4; ++n) ldwo(n);
    vmwait<8>();
    bar();
    {
      LAS char* const wq = opq(L + L_WQ + h * 512 + q4 * 64 + lsub + w * 1024);
      f32x4 c0 = {0.f, 0.f, 0.f, 0.f}, c1 = c0;
      v4u fq[8];
      UNR for (int c = 0; c < 8; ++c) fq[c] = trfrag(wq + c * 4096, 256);
      SB();
#pragma unroll 1
      for (int n = 4; n < 8; ++n) ldwo(n);
      __builtin_amdgcn_s_setprio(2);
      UNR for (int c = 0; c < 8; c += 2) { c0 = sum8(fq[c], ones, c0); c1 = sum8(fq[c + 1], ones, c1); }
      *(LAS float*)(L + L_CSQ + w * 256 + lane * 4) = c0[0] + c1[0];
      __builtin_amdgcn_s_setprio(0);
#pragma unroll 1
      for (int n = 8; n < WO1; ++n) ldwo(n);
    }
    vmwait<WO1>();
    bar();
    flag(OFF_F2 + (rb1 * 32 + hp) * 128, OFF_F2 + (rb * 32 + 16 + m) * 128);
    fill(5); fill(6);
    bar();
    { const u32 s = (rb * 128 + th * 64 + w * 16) * 1024 + cg * 128; glds(lb + l_xres + w * 2048, vx0, rx, s); glds_o<1024>(vx1, rx, s); }
    lda_sc1(fl, vf8, rws, OFF_F2 + rb * 4096);
#pragma unroll 1
    for (int n = WO1; n < 16; ++n) ldwo(n);
    ldw1(0);
    vmwait<19 - WO1>();
    flags_ok(fl, vf8, OFF_F2 + rb * 4096);
    const u32 wa1 = 65536 + w * 1024 + h * 512 + q4 * 64 + lsub;
    f32x4 cu[3];
    UNR for (int j = 0; j < 3; ++j) cu[j] = (f32x4){0.f, 0.f, 0.f, 0.f};
    auto cs1 = [&](int n) {
      LAS char* const a = opq(L + wa1 + ((n - rh) & 7) * 12288);
      v4u f[6];
      UNR for (int j = 0; j < 6; ++j) f[j] = trfrag(a + j * 4096, 256);
      SB();
      UNR for (int j = 0; j < 6; ++j) cu[j % 3] = sum8(f[j], ones, cu[j % 3]);
      UNR for (int j = 0; j < 3; ++j) PIN(cu[j]);
    };
    bar();
    ldw1(1); vmwait<6>();
    bar();
    ldw1(2); vmwait<3>(); ldw1(3);
    fill(7); fill(8);
    cs1(0);
    bar();
    u32 bb;
    asm volatile("buffer_load_ushort %0, %1, %2, %3 offen" : "=v"(bb) : "v"(h ? 1376 * 2 : t * 2), "s"(__builtin_amdgcn_make_buffer_rsrc((void*)p.b1, 0, 1376 * 2, 0x00020000)), "s"(min(u0 + w, 42) * 64) : "memory");
    asm volatile("buffer_load_dword %0, %1, %2, %3 offen" : "=v"(pf) : "v"(lane < (w < 3 ? 11 : 10) ? lane * 1024 : 1 << 21), "s"(r2), "s"((ib * 43 + w * 11) * 1024 + 384) : "memory");
    fill(9); fill(10);
    ldw1(4); ldw1(5);
    vmwait<10>();
    cs1(2);
    PAD(7, r);
    bar();
    vmwait<6>();
    PIN(pf);
    bar();
    flag(OFF_F3 + (rb * 16 + m3) * 128, b == 0 ? OFF_E : WS_BYTES);
    u32 lw = lb + w * 1024;
    asm volatile("" : "+s"(lw));
    auto ldw2 = [&](int q0, int q1) {
      UNR for (int q = q0; q < q1; ++q) glds2(lw, w2_base(q), vo, r2, s2 + (ubase(q & 15) + (q >> 4)) * 32768);
    };
    ldw1(6); ldw1(7);
    ldw2(0, QB0);
    vmwait<QB0 + 7>();
    cs1(4);
    vmwait<QB0>();
    cs1(6);
    UNR for (int j = 0; j < 3; ++j) *(LAS float*)(L + L_CS1 + j * 1024 + w * 256 + lane * 4) = cu[j][0];
    bar();
    {
      v4u sv[4]; u32 spins = 0;
      const u32 so = OFF_HS + (rb * 4 + w) * 4096;
      UNR for (int i = 0; i < 4; ++i) lda_sc1(sv[i], l16, rws, so + i * 1024);
      lda_sc1(fl, vf4, rws, OFF_F3 + rb * 2048);
      {
        LAS char* const cp = opq(L + L_CS1 + min(w, 2) * 1024 + lane * 4);
        PIN(bb);
        const v4u ag = aug_a((*(LAS float*)cp + *(LAS float*)(cp + 256)) + (*(LAS float*)(cp + 512) + *(LAS float*)(cp + 768)), bb);
        if (w < 3) *(LAS v4u*)(L + L_AUG + w * 1024 + l16) = ag;
      }
      for (;;) {
        vmwait<0>();
        u32 mm = 0;
        UNR for (int i = 0; i < 4; ++i) { PIN(sv[i]); mm = mx4(mm, sv[i]); }
        if (!anybad(mm) || ++spins >= SPIN_CAP) break;
        UNR for (int i = 0; i < 4; ++i) lda_sc1(sv[i], l16, rws, so + i * 1024);
      }
      flags_ok(fl, vf4, OFF_F3 + rb * 2048);
      f32x4 a;
      UNR for (int k = 0; k < 4; ++k) {
        float x = fbits(sv[0][k]) + fbits(sv[1][k]); PIN(x); x += fbits(sv[2][k]); PIN(x); x += fbits(sv[3][k]); PIN(x);
        x = xadd32(xadd16(x)) * (1.f / 512.f); PIN(x);
        a[k] = x;
      }
      if (lane < 16) *(LAS f32x4*)(L + L_STAT + w * 256 + l16) = a;
    }
    bar();
    ldw2(QB0, QB15);
    vmwait<QB15 - 3>();
    bar();
    ldw2(QB15, QB16);
    v4u st[NST + 1];
    UNR for (int j = 0; j < NST; ++j) lda(st[j], vo, r2, s2 + (ubase(11 - NST + j) + 2) * 32768);
    vmwait<8 + NST>();
    bar();
    ldw2(QB16, 43 - NST);
    sfor<0, NST>([&](auto J) {
      constexpr int j = J.value;
      vmwait<11 - j>();
      pinv(st[j]);
      *(LAS v4u*)(L + w2_base(43 - NST + j) + w * 1024 + l16) = st[j];
    });
    vmwait<0>();
    bar();
  }
}

extern "C" void kernel_launch(void* const* d_in, const int* in_sizes, int n_in, void* d_out, int out_size, void* d_ws, size_t ws_size,
                              hipStream_t stream) {
  Params p;
  p.x = (const u16*)d_in[0]; p.wqkv = (const u16*)d_in[1]; p.wphi = (const u16*)d_in[2]; p.wo = (const u16*)d_in[3];
  p.w1 = (const u16*)d_in[5]; p.b1 = (const u16*)d_in[6]; p.w2 = (const u16*)d_in[7]; p.b2 = (const u16*)d_in[8];
  p.out = (u16*)d_out; p.ws = (char*)d_ws;
  fused_block<<<256, 512, 0, stream>>>(p);
}
```
